```python
import torch

def _softmax(x, axis=-1):
    return torch.softmax(x, dim=axis)
def _gelu(x):
    return torch.nn.functional.gelu(x, approximate="tanh")
def _silu(x):
    return torch.nn.functional.silu(x)
def _sigmoid(x):
    return torch.sigmoid(x)
def _layernorm(x, axis=-1, eps=1e-5):
    m = x.mean(dim=axis, keepdim=True)
    v = x.var(dim=axis, keepdim=True, unbiased=False)
    return (x - m) / torch.sqrt(v + eps)
def _scatter_add(tgt, idx, src, axis=0):
    return tgt.clone().index_add_(axis, idx, src)
def _segment_sum(src, seg, n):
    out = torch.zeros((n,) + src.shape[1:], dtype=src.dtype)
    return out.index_add_(0, seg, src)
def _pad(x, pw):
    flat = tuple(v for pair in reversed(pw) for v in pair)
    return torch.nn.functional.pad(x, flat)

kernel_name = 'interleave_layers__linear_kernel_hedgehog__dense_activation_squared_relu__prompt'

def setup_inputs():

    _w = (lambda a: (lambda t: t.to(torch.bfloat16) if t.is_floating_point() else t)(torch.as_tensor(a)))
    def _A_setup():
        B, S, D, H = 1, 2048, 576, 32
        Dh = D // H
        return {
            "x": _w((lambda *s: torch.randn(*s, dtype=torch.bfloat16) * 0.05)(B, S, D)),
            "wqkv": _w((lambda *s: torch.randn(*s, dtype=torch.bfloat16) * 0.2)(D, 3 * D)),
            "w_phi": _w((lambda *s: torch.randn(*s, dtype=torch.bfloat16) * 0.2)(H, Dh, Dh) * (Dh ** -0.5)),
            "wo": _w((lambda *s: torch.randn(*s, dtype=torch.bfloat16) * 0.1)(D, D)),
        }
    _A_in = _A_setup()

    def _B_setup():
        B, S, D, F = 1, 2048, 576, 4608
        return {
            "x": _w((lambda *s: torch.randn(*s, dtype=torch.bfloat16) * 0.05)(B, S, D)),
            "w1": _w((lambda *s: torch.randn(*s, dtype=torch.bfloat16) * 0.05)(D, F)),
            "b1": _w((lambda *s: torch.randn(*s, dtype=torch.bfloat16) * 0.2)(F)),
            "w2": _w((lambda *s: torch.randn(*s, dtype=torch.bfloat16) * 0.05)(F, D)),
            "b2": _w((lambda *s: torch.randn(*s, dtype=torch.bfloat16) * 1.6)(D)),
        }
    _B_in = _B_setup()

    return {
        "A_x": _A_in["x"],
        "A_wqkv": _A_in["wqkv"],
        "A_w_phi": _A_in["w_phi"],
        "A_wo": _A_in["wo"],
        "B_x": _B_in["x"],
        "B_w1": _B_in["w1"],
        "B_b1": _B_in["b1"],
        "B_w2": _B_in["w2"],
        "B_b2": _B_in["b2"],
    }

def reference(A_x, A_wqkv, A_w_phi, A_wo, B_x, B_w1, B_b1, B_w2, B_b2):
    def _A_ref(x, wqkv, w_phi, wo):
        B, S, D = x.shape
        H = 32; Dh = D // H
        qkv = (lambda x, *d: x.permute(*d))((x @ wqkv).reshape(B, S, 3, H, Dh), 2, 0, 3, 1, 4)
        q, k, v = qkv[0], qkv[1], qkv[2]
        pq = torch.einsum("bhsd,hdf->bhsf", q, w_phi)
        pk = torch.einsum("bhsd,hdf->bhsf", k, w_phi)
        phi_q = torch.concatenate([_softmax(pq, axis=-1), _softmax(-pq, axis=-1)], axis=-1)
        phi_k = torch.concatenate([_softmax(pk, axis=-1), _softmax(-pk, axis=-1)], axis=-1)
        kv = torch.einsum("bhsf,bhsd->bhfd", phi_k, v)
        z  = torch.einsum("bhsf->bhf", phi_k)
        num = torch.einsum("bhqf,bhfd->bhqd", phi_q, kv)
        den = torch.einsum("bhqf,bhf->bhq", phi_q, z)[..., None] + 1e-6
        o = num / den
        return (lambda x, *d: x.permute(*d))(o, 0, 2, 1, 3).reshape(B, S, D) @ wo

    def _B_ref(x, w1, b1, w2, b2):
        h = torch.einsum("bsd,df->bsf", x, w1) + b1
        zero = torch.zeros_like(h)
        r = torch.where(h > zero, h, zero)
        h = r * r
        out = torch.einsum("bsf,fd->bsd", h, w2) + b2
        return out

    x = A_x
    x = x + _A_ref(_layernorm(x, axis=-1), A_wqkv, A_w_phi, A_wo)
    x = x + _B_ref(_layernorm(x, axis=-1), B_w1, B_b1, B_w2, B_b2)
    x = x + _A_ref(_layernorm(x, axis=-1), A_wqkv, A_w_phi, A_wo)
    x = x + _B_ref(_layernorm(x, axis=-1), B_w1, B_b1, B_w2, B_b2)
    return x
```

```cpp
#include <hip/hip_runtime.h>
#include <hip/hip_bf16.h>
#include <stdint.h>

typedef unsigned short u16;
constexpr int S_ = 2048, D_ = 576, H_ = 32, DH = 18, F_ = 4608, N3 = 1728;

__device__ __forceinline__ float bf2f(u16 v) { return __uint_as_float(((unsigned)v) << 16); }
__device__ __forceinline__ u16 f2bf(float f) {
  __hip_bfloat16 b = __float2bfloat16(f);
  return *reinterpret_cast<u16*>(&b);
}
__device__ __forceinline__ float rbf(float f) { return bf2f(f2bf(f)); }

__global__ void ln_k(const u16* x, u16* xn) {
  int r = blockIdx.x, t = threadIdx.x;
  float v[9]; float s = 0;
  for (int i = 0; i < 9; i++) { v[i] = bf2f(x[r * D_ + t + 64 * i]); s += v[i]; }
  for (int o = 32; o; o >>= 1) s += __shfl_xor(s, o);
  float m = s / D_;
  float q = 0;
  for (int i = 0; i < 9; i++) { float d = v[i] - m; q += d * d; }
  for (int o = 32; o; o >>= 1) q += __shfl_xor(q, o);
  float var = rbf(q / D_);
  float mb = rbf(m);
  float sd = rbf(sqrtf(rbf(var + 1e-5f)));
  for (int i = 0; i < 9; i++) xn[r * D_ + t + 64 * i] = f2bf(rbf(v[i] - mb) / sd);
}

__global__ void gemm_k(const u16* A, const u16* B, u16* C, int M, int N, int K, int mode, const u16* bias, const u16* resid) {
  __shared__ float sA[16][17], sB[16][17];
  int tx = threadIdx.x, ty = threadIdx.y;
  int row = blockIdx.y * 16 + ty, col = blockIdx.x * 16 + tx;
  float acc = 0;
  for (int k0 = 0; k0 < K; k0 += 16) {
    sA[ty][tx] = bf2f(A[(size_t)row * K + k0 + tx]);
    sB[ty][tx] = bf2f(B[(size_t)(k0 + ty) * N + col]);
    __syncthreads();
    for (int k = 0; k < 16; k++) acc += sA[ty][k] * sB[k][tx];
    __syncthreads();
  }
  float r = rbf(acc);
  if (mode == 1) { r = rbf(r + bf2f(bias[col])); r = r > 0 ? r : 0; r = rbf(r * r); }
  else if (mode == 2) { if (bias) r = rbf(r + bf2f(bias[col])); r = rbf(bf2f(resid[(size_t)row * N + col]) + r); }
  C[(size_t)row * N + col] = f2bf(r);
}

__global__ __launch_bounds__(256) void phi_k(const u16* qkv, const u16* wphi, u16* phiq, u16* phik) {
  int idx = blockIdx.x * blockDim.x + threadIdx.x;
  if (idx >= S_ * H_ * 2) return;
  int which = idx % 2, h = (idx / 2) % H_, s = idx / (2 * H_);
  const u16* src = qkv + (size_t)s * N3 + which * D_ + h * DH;
  float q[DH], p[DH];
  #pragma unroll
  for (int d = 0; d < DH; d++) q[d] = bf2f(src[d]);
  float mx = -1e30f, mn = 1e30f;
  #pragma unroll
  for (int f = 0; f < DH; f++) {
    float a = 0;
    #pragma unroll
    for (int d = 0; d < DH; d++) a += q[d] * bf2f(wphi[(h * DH + d) * DH + f]);
    p[f] = rbf(a); mx = fmaxf(mx, p[f]); mn = fminf(mn, p[f]);
  }
  float s1 = 0, s2 = 0, e1[DH], e2[DH];
  #pragma unroll
  for (int f = 0; f < DH; f++) { e1[f] = expf(p[f] - mx); s1 += e1[f]; e2[f] = expf(mn - p[f]); s2 += e2[f]; }
  u16* dst = (which ? phik : phiq) + ((size_t)s * H_ + h) * 36;
  #pragma unroll
  for (int f = 0; f < DH; f++) { dst[f] = f2bf(e1[f] / s1); dst[DH + f] = f2bf(e2[f] / s2); }
}

__global__ void kv_k(const u16* phik, const u16* qkv, float* kv) {
  int idx = blockIdx.x * blockDim.x + threadIdx.x;
  if (idx >= H_ * 36 * 19) return;
  int d = idx % 19, f = (idx / 19) % 36, h = idx / (19 * 36);
  float a = 0;
  for (int s = 0; s < S_; s++) {
    float p = bf2f(phik[((size_t)s * H_ + h) * 36 + f]);
    float v = d < 18 ? bf2f(qkv[(size_t)s * N3 + 2 * D_ + h * DH + d]) : 1.f;
    a += p * v;
  }
  kv[idx] = rbf(a);
}

__global__ void o_k(const u16* phiq, const float* kv, u16* o) {
  int idx = blockIdx.x * blockDim.x + threadIdx.x;
  if (idx >= S_ * H_) return;
  int h = idx % H_, s = idx / H_;
  float num[19];
  for (int d = 0; d < 19; d++) num[d] = 0;
  for (int f = 0; f < 36; f++) {
    float p = bf2f(phiq[((size_t)s * H_ + h) * 36 + f]);
    for (int d = 0; d < 19; d++) num[d] += p * kv[(h * 36 + f) * 19 + d];
  }
  float den = rbf(rbf(num[18]) + 1e-6f);
  for (int d = 0; d < 18; d++) o[(size_t)s * D_ + h * DH + d] = f2bf(rbf(num[d]) / den);
}

extern "C" void kernel_launch(void* const* d_in, const int* in_sizes, int n_in,
                              void* d_out, int out_size, void* d_ws, size_t ws_size,
                              hipStream_t stream) {
  const u16* x0 = (const u16*)d_in[0];
  const u16* wqkv = (const u16*)d_in[1];
  const u16* wphi = (const u16*)d_in[2];
  const u16* wo = (const u16*)d_in[3];
  const u16* w1 = (const u16*)d_in[5];
  const u16* b1 = (const u16*)d_in[6];
  const u16* w2 = (const u16*)d_in[7];
  const u16* b2 = (const u16*)d_in[8];
  char* ws = (char*)d_ws;
  size_t off = 0;
  auto alloc = [&](size_t b) { void* p = ws + off; off += (b + 255) & ~(size_t)255; return p; };
  u16* xn = (u16*)alloc(S_ * D_ * 2);
  u16* qkv = (u16*)alloc((size_t)S_ * N3 * 2);
  u16* phiq = (u16*)alloc((size_t)S_ * H_ * 36 * 2);
  u16* phik = (u16*)alloc((size_t)S_ * H_ * 36 * 2);
  float* kv = (float*)alloc(H_ * 36 * 19 * 4);
  u16* o = (u16*)alloc(S_ * D_ * 2);
  u16* xa = (u16*)alloc(S_ * D_ * 2);
  u16* xb = (u16*)alloc(S_ * D_ * 2);
  u16* h = (u16*)alloc((size_t)S_ * F_ * 2);
  const u16* xin = x0;
  for (int l = 0; l < 2; l++) {
    ln_k<<<S_, 64, 0, stream>>>(xin, xn);
    gemm_k<<<dim3(N3 / 16, S_ / 16), dim3(16, 16), 0, stream>>>(xn, wqkv, qkv, S_, N3, D_, 0, nullptr, nullptr);
    phi_k<<<(S_ * H_ * 2 + 255) / 256, 256, 0, stream>>>(qkv, wphi, phiq, phik);
    kv_k<<<(H_ * 36 * 19 + 255) / 256, 256, 0, stream>>>(phik, qkv, kv);
    o_k<<<(S_ * H_ + 255) / 256, 256, 0, stream>>>(phiq, kv, o);
    gemm_k<<<dim3(D_ / 16, S_ / 16), dim3(16, 16), 0, stream>>>(o, wo, xa, S_, D_, D_, 2, nullptr, xin);
    ln_k<<<S_, 64, 0, stream>>>(xa, xn);
    gemm_k<<<dim3(F_ / 16, S_ / 16), dim3(16, 16), 0, stream>>>(xn, w1, h, S_, F_, D_, 1, b1, nullptr);
    u16* dst = l == 0 ? xb : (u16*)d_out;
    gemm_k<<<dim3(D_ / 16, S_ / 16), dim3(16, 16), 0, stream>>>(h, w2, dst, S_, D_, F_, 2, b2, xa);
    xin = xb;
  }
}
```

```cpp
#include <hip/hip_runtime.h>
#include <stdint.h>

typedef unsigned short u16;
typedef __attribute__((ext_vector_type(4))) unsigned u32x4;
typedef __attribute__((ext_vector_type(2))) unsigned u32x2;
typedef __attribute__((ext_vector_type(4))) float f32x4;
typedef __attribute__((ext_vector_type(2))) float f32x2;
typedef __attribute__((ext_vector_type(8))) __bf16 bf16x8;
typedef __attribute__((ext_vector_type(2))) __bf16 bf16x2;

constexpr int S_ = 2048, D_ = 576, F_ = 4608, N3 = 1728;
constexpr int TILE_B = 73728;
constexpr size_t ACT_B = (size_t)S_ * D_ * 2;

__device__ __forceinline__ unsigned pk2(float a, float b) {
  f32x2 v = {a, b};
  bf16x2 r = __builtin_convertvector(v, bf16x2);
  return __builtin_bit_cast(unsigned, r);
}
__device__ __forceinline__ float blo(unsigned u) { return __uint_as_float(u << 16); }
__device__ __forceinline__ float bhi(unsigned u) { return __uint_as_float(u & 0xffff0000u); }
__device__ __forceinline__ float b2f(u16 v) { return __uint_as_float(((unsigned)v) << 16); }
__device__ __forceinline__ f32x4 mfma16(u32x4 a, u32x4 b, f32x4 c) {
  return __builtin_amdgcn_mfma_f32_16x16x32_bf16(__builtin_bit_cast(bf16x8, a), __builtin_bit_cast(bf16x8, b), c, 0, 0, 0);
}
__device__ __forceinline__ void glds16(const void* g, void* l) {
  unsigned la = __builtin_amdgcn_readfirstlane((unsigned)(uintptr_t)(__attribute__((address_space(3))) char*)l);
  asm volatile("s_mov_b32 m0, %0\n\ts_nop 0\n\tglobal_load_lds_dwordx4 %1, off" ::"s"(la), "v"(g) : "memory");
}
__device__ __forceinline__ void bar() {
  asm volatile("s_waitcnt lgkmcnt(0)" ::: "memory");
  __builtin_amdgcn_s_barrier();
  asm volatile("" ::: "memory");
}

template <int MODE>
__device__ __forceinline__ void red_body(int bid, const u16* __restrict__ xin, const u16* __restrict__ P,
                                         const u16* __restrict__ b2, u16* __restrict__ xf,
                                         u16* __restrict__ xn, u16* __restrict__ out, f32x2* st) {
  const int tid = threadIdx.x, w = tid >> 6, l = tid & 63;
  const int rt = bid >> 3, m = (bid & 7) >> 1, rh = bid & 1;
  const int ks = 2 * w + (l >> 5), kq = (l >> 3) & 3, r = l & 7;
  const int row = rt * 64 + m * 16 + rh * 8 + r, col = ks * 32 + kq * 8;
  const size_t fo = ((size_t)(((rt * 18 + ks) * 4 + m) * 64 + kq * 16 + rh * 8 + r)) * 8;
  float v[8];
  if (MODE == 0) {
    u32x4 a = *(const u32x4*)(xin + (size_t)row * D_ + col);
#pragma unroll
    for (int i = 0; i < 4; i++) { v[2 * i] = blo(a[i]); v[2 * i + 1] = bhi(a[i]); }
    *(u32x4*)(xf + fo) = a;
  } else {
    u32x4 p[8];
#pragma unroll
    for (int c = 0; c < 8; c++) p[c] = *(const u32x4*)(P + (size_t)c * (ACT_B / 2) + fo);
    u32x4 a = *(const u32x4*)(xin + fo);
    u32x4 bb = *(const u32x4*)(b2 + col);
#pragma unroll
    for (int i = 0; i < 4; i++) {
      float s0 = 0.f, s1 = 0.f;
#pragma unroll
      for (int c = 0; c < 8; c++) { s0 += blo(p[c][i]); s1 += bhi(p[c][i]); }
      s0 += blo(bb[i]); s1 += bhi(bb[i]);
      s0 += blo(a[i]); s1 += bhi(a[i]);
      unsigned q = pk2(s0, s1);
      a[i] = q;
      v[2 * i] = blo(q); v[2 * i + 1] = bhi(q);
    }
    if (MODE == 2) { *(u32x4*)(out + (size_t)row * D_ + col) = a; return; }
    *(u32x4*)(xf + fo) = a;
  }
  float s = 0.f;
#pragma unroll
  for (int i = 0; i < 8; i++) s += v[i];
  float mu = s * 0.125f, m2 = 0.f;
#pragma unroll
  for (int i = 0; i < 8; i++) { float d = v[i] - mu; m2 += d * d; }
  float cnt = 8.f;
#pragma unroll
  for (int o = 8; o <= 32; o <<= 1) {
    float mu2 = __shfl_xor(mu, o), m22 = __shfl_xor(m2, o);
    float d = mu - mu2;
    m2 = m2 + m22 + d * d * (cnt * 0.5f);
    mu = 0.5f * (mu + mu2);
    cnt *= 2.f;
  }
  if (l < 8) { f32x2 t = {mu, m2}; st[w * 8 + l] = t; }
  __syncthreads();
  float mus[9], msum = 0.f, m2s = 0.f;
#pragma unroll
  for (int i = 0; i < 9; i++) { f32x2 t = st[i * 8 + r]; mus[i] = t[0]; msum += t[0]; m2s += t[1]; }
  float mean = msum * (1.f / 9.f);
#pragma unroll
  for (int i = 0; i < 9; i++) { float d = mus[i] - mean; m2s += 64.f * d * d; }
  float rstd = 1.0f / sqrtf(m2s * (1.f / 576.f) + 1e-5f);
  u32x4 o;
#pragma unroll
  for (int i = 0; i < 4; i++) o[i] = pk2((v[2 * i] - mean) * rstd, (v[2 * i + 1] - mean) * rstd);
  *(u32x4*)(xn + fo) = o;
}

template <int MODE>
__global__ __launch_bounds__(576) void red_k(const u16* __restrict__ xin, const u16* __restrict__ P,
                                              const u16* __restrict__ b2, u16* __restrict__ xf,
                                              u16* __restrict__ xn, u16* __restrict__ out) {
  __shared__ f32x2 st[72];
  red_body<MODE>(blockIdx.x, xin, P, b2, xf, xn, out, st);
}

__device__ __forceinline__ void tr_store(const u32x2* r, u16* dst) {
  u32x4 o0, o1, o2, o3;
#pragma unroll
  for (int p = 0; p < 4; p++) {
    o0[p] = __builtin_amdgcn_perm(r[2 * p + 1][0], r[2 * p][0], 0x05040100u);
    o1[p] = __builtin_amdgcn_perm(r[2 * p + 1][0], r[2 * p][0], 0x07060302u);
    o2[p] = __builtin_amdgcn_perm(r[2 * p + 1][1], r[2 * p][1], 0x05040100u);
    o3[p] = __builtin_amdgcn_perm(r[2 * p + 1][1], r[2 * p][1], 0x07060302u);
  }
  u32x4* d = (u32x4*)dst;
  d[0] = o0; d[1] = o1; d[2] = o2; d[3] = o3;
}

constexpr int T_FOLD = 1296, T_WV = 180, T_WO = 180, T_W1 = 1296, T_W2 = 1296;
constexpr int T_ALL = T_FOLD + T_WV + T_WO + T_W1 + T_W2;
constexpr int PREP_BLOCKS = 256 + (T_ALL + 8) / 9;

__global__ __launch_bounds__(576) void prep_k(const u16* __restrict__ x0, const u16* __restrict__ wqkv,
                                               const u16* __restrict__ wphi, const u16* __restrict__ wo,
                                               const u16* __restrict__ w1, const u16* __restrict__ w2,
                                               u16* __restrict__ xf, u16* __restrict__ xn,
                                               u16* __restrict__ WA, u16* __restrict__ WO, u16* __restrict__ WF) {
  __shared__ f32x2 st[72];
  if (blockIdx.x < 256) {
    red_body<0>(blockIdx.x, x0, nullptr, nullptr, xf, xn, nullptr, st);
    return;
  }
  const int w = __builtin_amdgcn_readfirstlane(threadIdx.x >> 6), l = threadIdx.x & 63;
  int t = (blockIdx.x - 256) * 9 + w;
  const int kq = l >> 4, cq = l & 15;
  if (t < T_FOLD) {
    const int tt = t % 9, hg = (t / 9) & 7, ks = t / 72;
    const int b0 = (16 * tt) / 18, b1 = (16 * tt + 15) / 18;
    const int vc = 16 * tt + cq, bc = vc / 18, fc = vc - bc * 18;
    f32x4 acc0 = {0, 0, 0, 0}, acc1 = {0, 0, 0, 0};
    for (int b = b0; b <= b1; b++) {
      const int head = hg * 4 + (b & 3);
      const int colb = (b < 4 ? 0 : 576) + head * 18;
      u32x4 bf = {0, 0, 0, 0};
      if (bc == b && kq < 3) {
        const u16* wp = wphi + (head * 18 + 8 * kq) * 18 + fc;
        unsigned e[8];
#pragma unroll
        for (int j = 0; j < 8; j++) e[j] = (8 * kq + j < 18) ? (unsigned)wp[j * 18] : 0u;
#pragma unroll
        for (int p = 0; p < 4; p++) bf[p] = e[2 * p] | (e[2 * p + 1] << 16);
      }
      u32x4 a0 = {0, 0, 0, 0}, a1 = {0, 0, 0, 0};
      if (kq < 3) {
        const int kr = ks * 32 + 8 * (cq >> 2) + (cq & 3);
        const unsigned* s0 = (const unsigned*)(wqkv + (size_t)kr * N3 + colb + 8 * kq);
        const unsigned* s1 = (const unsigned*)(wqkv + (size_t)(kr + 4) * N3 + colb + 8 * kq);
        a0[0] = s0[0]; a1[0] = s1[0];
        if (kq < 2) {
#pragma unroll
          for (int p = 1; p < 4; p++) { a0[p] = s0[p]; a1[p] = s1[p]; }
        }
      }
      acc0 = mfma16(a0, bf, acc0);
      acc1 = mfma16(a1, bf, acc1);
    }
    u32x4 o = {pk2(acc0[0], acc0[1]), pk2(acc0[2], acc0[3]), pk2(acc1[0], acc1[1]), pk2(acc1[2], acc1[3])};
    *(u32x4*)(WA + ((size_t)((hg * 18 + ks) * 14 + tt) * 64 + l) * 8) = o;
    return;
  }
  t -= T_FOLD;
  u32x2 r[8];
  if (t < T_WV + T_WO) {
    const bool isV = t < T_WV;
    if (!isV) t -= T_WV;
    const int ks = t / 10, g = (t % 10) * 16 + cq, cg = g / 20, q = g % 20;
    if (q < 18) {
#pragma unroll
      for (int j = 0; j < 8; j++) {
        const int kp = ks * 32 + kq * 8 + j;
        if (isV) {
          r[j] = *(const u32x2*)(wqkv + (size_t)kp * N3 + 1152 + cg * 72 + 4 * q);
        } else {
          int row = kp < 512 ? (kp >> 4) * 18 + (kp & 15) : ((kp - 512) >> 1) * 18 + 16 + (kp & 1);
          r[j] = *(const u32x2*)(wo + (size_t)row * D_ + cg * 72 + 4 * q);
        }
      }
    } else {
#pragma unroll
      for (int j = 0; j < 8; j++) { r[j][0] = 0; r[j][1] = 0; }
    }
    const int nt = q >> 2, c15 = 4 * (q & 3);
    u16* dst = isV ? WA + ((size_t)((cg * 18 + ks) * 14 + 9 + nt) * 64 + kq * 16 + c15) * 8
                   : WO + ((size_t)((cg * 18 + ks) * 5 + nt) * 64 + kq * 16 + c15) * 8;
    tr_store(r, dst);
    return;
  }
  t -= T_WV + T_WO;
  if (t < T_W1) {
    const int ks = t / 72, cb = t % 72, c = cb / 9, nt = (cb % 9) * 4 + (cq >> 2);
    const u16* src = w1 + (size_t)(ks * 32 + kq * 8) * F_ + cb * 64 + 4 * cq;
#pragma unroll
    for (int j = 0; j < 8; j++) r[j] = *(const u32x2*)(src + (size_t)j * F_);
    tr_store(r, WF + ((size_t)((c * 36 + ks) * 36 + nt) * 64 + kq * 16 + 4 * (cq & 3)) * 8);
    return;
  }
  t -= T_W1;
  if (t < T_W2) {
    const int kb = t / 9, cb = t % 9, c = kb / 18, step = 18 + kb % 18, nt = cb * 4 + (cq >> 2);
    const u16* src = w2 + (size_t)(kb * 32 + kq * 8) * D_ + cb * 64 + 4 * cq;
#pragma unroll
    for (int j = 0; j < 8; j++) r[j] = *(const u32x2*)(src + (size_t)j * D_);
    tr_store(r, WF + ((size_t)((c * 36 + step) * 36 + nt) * 64 + kq * 16 + 4 * (cq & 3)) * 8);
  }
}

constexpr int PK_STR = 68;
constexpr int VT_STR = 72;
__global__ __launch_bounds__(448) void k1_k(const u16* __restrict__ xn, const u16* __restrict__ WA,
                                             u16* __restrict__ pqT, float* __restrict__ kvp) {
  __shared__ __attribute__((aligned(16))) char sA[TILE_B];
  __shared__ __attribute__((aligned(16))) float sPk[72 * PK_STR];
  __shared__ __attribute__((aligned(16))) u16 sV[73 * VT_STR];
  const int tid = threadIdx.x, w = __builtin_amdgcn_readfirstlane(tid >> 6), l = tid & 63;
  const int hg = blockIdx.x & 7, rt = blockIdx.x >> 3;
  const int c15 = l & 15, g = l >> 4;
  {
    const char* src = (const char*)xn + (size_t)rt * TILE_B + l * 16;
    for (int i = w; i < 72; i += 7) glds16(src + i * 1024, sA + i * 1024);
  }
  u32x4 bw[18][2];
  {
    const char* wp = (const char*)WA + (size_t)(hg * 18 * 14 + 2 * w) * 1024 + l * 16;
#pragma unroll
    for (int ks = 0; ks < 18; ks++) {
      bw[ks][0] = *(const u32x4*)(wp + ks * 14336);
      bw[ks][1] = *(const u32x4*)(wp + ks * 14336 + 1024);
    }
  }
  if (w == 6) { ((unsigned*)(sV + 72 * VT_STR))[l & 31] = 0x3F803F80u; }
  asm volatile("s_waitcnt vmcnt(36)" ::: "memory");
  bar();
  f32x4 acc[2][4];
#pragma unroll
  for (int i = 0; i < 2; i++)
#pragma unroll
    for (int m = 0; m < 4; m++) acc[i][m] = (f32x4){0, 0, 0, 0};
#pragma unroll
  for (int ks = 0; ks < 18; ks++) {
    u32x4 a[4];
#pragma unroll
    for (int m = 0; m < 4; m++) a[m] = *(const u32x4*)(sA + (ks * 4 + m) * 1024 + l * 16);
#pragma unroll
    for (int i = 0; i < 2; i++)
#pragma unroll
      for (int m = 0; m < 4; m++) acc[i][m] = mfma16(a[m], bw[ks][i], acc[i][m]);
  }
#pragma unroll
  for (int i = 0; i < 2; i++) {
    const int vc = (2 * w + i) * 16 + c15;
    if (vc < 72) {
      u16* dst = pqT + ((size_t)(rt * 576 + hg * 72 + vc)) * 64 + 4 * g;
#pragma unroll
      for (int m = 0; m < 4; m++) {
        u32x2 o = {pk2(acc[i][m][0], acc[i][m][1]), pk2(acc[i][m][2], acc[i][m][3])};
        *(u32x2*)(dst + m * 16) = o;
      }
    } else if (vc < 144) {
#pragma unroll
      for (int m = 0; m < 4; m++) *(f32x4*)(sPk + (vc - 72) * PK_STR + m * 16 + 4 * g) = acc[i][m];
    } else if (vc < 216) {
#pragma unroll
      for (int m = 0; m < 4; m++) {
        u32x2 o = {pk2(acc[i][m][0], acc[i][m][1]), pk2(acc[i][m][2], acc[i][m][3])};
        *(u32x2*)(sV + (vc - 144) * VT_STR + m * 16 + 4 * g) = o;
      }
    }
  }
  bar();
  u16* sPhi = (u16*)sA;
  if (w < 4) {
    float p[18];
#pragma unroll
    for (int f = 0; f < 18; f++) p[f] = sPk[(18 * w + f) * PK_STR + l];
    float mx = p[0], mn = p[0];
#pragma unroll
    for (int f = 1; f < 18; f++) { mx = fmaxf(mx, p[f]); mn = fminf(mn, p[f]); }
    float e1[18], e2[18], s1 = 0.f, s2 = 0.f;
#pragma unroll
    for (int f = 0; f < 18; f++) {
      e1[f] = __expf(p[f] - mx); s1 += e1[f];
      e2[f] = __expf(mn - p[f]); s2 += e2[f];
    }
    const float r1 = 1.0f / s1, r2 = 1.0f / s2;
    u16* dst = sPhi + w * 48 * VT_STR + l;
#pragma unroll
    for (int f = 0; f < 18; f += 2) {
      unsigned a = pk2(e1[f] * r1, e1[f + 1] * r1), b = pk2(e2[f] * r2, e2[f + 1] * r2);
      dst[f * VT_STR] = (u16)a; dst[(f + 1) * VT_STR] = (u16)(a >> 16);
      dst[(18 + f) * VT_STR] = (u16)b; dst[(19 + f) * VT_STR] = (u16)(b >> 16);
    }
    asm volatile("s_waitcnt lgkmcnt(0)" ::: "memory");
    const int vr0 = 18 * w + c15, vr1 = c15 < 2 ? 18 * w + 16 + c15 : 72;
    f32x4 d0[3], d1[3];
#pragma unroll
    for (int ft = 0; ft < 3; ft++) { d0[ft] = (f32x4){0, 0, 0, 0}; d1[ft] = (f32x4){0, 0, 0, 0}; }
#pragma unroll
    for (int ks = 0; ks < 2; ks++) {
      u32x4 va = *(const u32x4*)(sV + vr0 * VT_STR + ks * 32 + g * 8);
      u32x4 vb = *(const u32x4*)(sV + vr1 * VT_STR + ks * 32 + g * 8);
#pragma unroll
      for (int ft = 0; ft < 3; ft++) {
        u32x4 ph = *(const u32x4*)(sPhi + (w * 48 + ft * 16 + c15) * VT_STR + ks * 32 + g * 8);
        d0[ft] = mfma16(va, ph, d0[ft]);
        d1[ft] = mfma16(vb, ph, d1[ft]);
      }
    }
    float* dst2 = kvp + (size_t)((hg * 4 + w) * 32 + rt) * 720;
#pragma unroll
    for (int ft = 0; ft < 3; ft++) {
      const int f = ft * 16 + c15;
      if (f < 36) {
        *(f32x4*)(dst2 + f * 20 + 4 * g) = d0[ft];
        if (g == 0) *(f32x4*)(dst2 + f * 20 + 16) = d1[ft];
      }
    }
  }
}

__global__ __launch_bounds__(256) void kr_k(const u16* __restrict__ pqT, const float* __restrict__ kvp,
                                             u16* __restrict__ of) {
  __shared__ __attribute__((aligned(16))) u16 sKv[32 * VT_STR];
  __shared__ __attribute__((aligned(16))) u16 sPhi[256 * VT_STR];
  const int tid = threadIdx.x, w = __builtin_amdgcn_readfirstlane(tid >> 6), l = tid & 63;
  const int hg = blockIdx.x & 7, idx = blockIdx.x >> 3, head = hg * 4 + (idx & 3), rt = (idx >> 2) * 4 + w;
  const int c15 = l & 15, g = l >> 4;
  f32x4 part[32];
  if (tid < 180) {
    const float* src = kvp + (size_t)head * 32 * 720 + 4 * tid;
#pragma unroll
    for (int r = 0; r < 32; r++) part[r] = *(const f32x4*)(src + r * 720);
  }
  u16 pr[18];
  {
    const u16* src = pqT + ((size_t)(rt * 576 + head * 18)) * 64 + l;
#pragma unroll
    for (int f = 0; f < 18; f++) pr[f] = src[f * 64];
  }
  for (int i = tid; i < 32 * 18; i += 256) ((unsigned*)(sKv + (i / 18) * VT_STR + 36))[i % 18] = 0u;
  {
    float p[18];
#pragma unroll
    for (int f = 0; f < 18; f++) p[f] = b2f(pr[f]);
    float mx = p[0], mn = p[0];
#pragma unroll
    for (int f = 1; f < 18; f++) { mx = fmaxf(mx, p[f]); mn = fminf(mn, p[f]); }
    float e1[18], e2[18], s1 = 0.f, s2 = 0.f;
#pragma unroll
    for (int f = 0; f < 18; f++) {
      e1[f] = __expf(p[f] - mx); s1 += e1[f];
      e2[f] = __expf(mn - p[f]); s2 += e2[f];
    }
    const float r1 = 1.0f / s1, r2 = 1.0f / s2;
    unsigned q[20];
#pragma unroll
    for (int f = 0; f < 9; f++) {
      q[f] = pk2(e1[2 * f] * r1, e1[2 * f + 1] * r1);
      q[9 + f] = pk2(e2[2 * f] * r2, e2[2 * f + 1] * r2);
    }
    q[18] = 0; q[19] = 0;
    u32x4* dst = (u32x4*)(sPhi + tid * VT_STR);
#pragma unroll
    for (int i = 0; i < 5; i++) dst[i] = (u32x4){q[4 * i], q[4 * i + 1], q[4 * i + 2], q[4 * i + 3]};
#pragma unroll
    for (int i = 5; i < 8; i++) dst[i] = (u32x4){0, 0, 0, 0};
  }
  if (tid < 180) {
    f32x4 s = part[0];
#pragma unroll
    for (int r = 1; r < 32; r++) s += part[r];
    const int f = tid / 5, d0 = 4 * (tid % 5);
    unsigned a = pk2(s[0], s[1]), b = pk2(s[2], s[3]);
    sKv[(d0 + 0) * VT_STR + f] = (u16)a; sKv[(d0 + 1) * VT_STR + f] = (u16)(a >> 16);
    sKv[(d0 + 2) * VT_STR + f] = (u16)b; sKv[(d0 + 3) * VT_STR + f] = (u16)(b >> 16);
  }
  bar();
  u32x4 ka[2][2];
#pragma unroll
  for (int dt = 0; dt < 2; dt++)
#pragma unroll
    for (int ks = 0; ks < 2; ks++) {
      const int d = dt == 0 ? c15 : (16 + (c15 & 3));
      ka[dt][ks] = *(const u32x4*)(sKv + d * VT_STR + ks * 32 + g * 8);
    }
  const int ksA = head >> 1, kqA = (head & 1) * 2 + (g >> 1);
  const int ksB = 16 + (head >> 4), kqB = (head & 15) >> 2, jB = 2 * (head & 3);
#pragma unroll
  for (int m = 0; m < 4; m++) {
    f32x4 d0 = {0, 0, 0, 0}, d1 = {0, 0, 0, 0};
#pragma unroll
    for (int ks = 0; ks < 2; ks++) {
      u32x4 ph = *(const u32x4*)(sPhi + (w * 64 + m * 16 + c15) * VT_STR + ks * 32 + g * 8);
      d0 = mfma16(ka[0][ks], ph, d0);
      d1 = mfma16(ka[1][ks], ph, d1);
    }
    const float rden = 1.0f / (d1[2] + 1e-6f);
    u32x2 o = {pk2(d0[0] * rden, d0[1] * rden), pk2(d0[2] * rden, d0[3] * rden)};
    *(u32x2*)(of + ((size_t)(((rt * 18 + ksA) * 4 + m) * 64 + kqA * 16 + c15)) * 8 + 4 * (g & 1)) = o;
    if (g == 0)
      *(unsigned*)(of + ((size_t)(((rt * 18 + ksB) * 4 + m) * 64 + kqB * 16 + c15)) * 8 + jB) = pk2(d1[0] * rden, d1[1] * rden);
  }
}

__global__ __launch_bounds__(320) void k2_k(const u16* __restrict__ of, const u16* __restrict__ WO,
                                             const u16* __restrict__ xf, u16* __restrict__ x1) {
  __shared__ __attribute__((aligned(16))) char sA[TILE_B];
  const int tid = threadIdx.x, w = __builtin_amdgcn_readfirstlane(tid >> 6), l = tid & 63;
  const int rt = (blockIdx.x & 7) * 4 + ((blockIdx.x >> 3) & 3), cg = blockIdx.x >> 5;
  const int c15 = l & 15, g = l >> 4;
  {
    const char* src = (const char*)of + (size_t)rt * TILE_B + l * 16;
    for (int i = w; i < 72; i += 5) glds16(src + i * 1024, sA + i * 1024);
  }
  u32x4 bw[18];
  {
    const char* wp = (const char*)WO + (size_t)(cg * 18 * 5 + w) * 1024 + l * 16;
#pragma unroll
    for (int ks = 0; ks < 18; ks++) bw[ks] = *(const u32x4*)(wp + ks * 5120);
  }
  const int cl = 16 * w + 4 * g;
  const bool valid = cl < 72;
  const int col = cg * 72 + (valid ? cl : 0);
  const size_t fo = ((size_t)(((rt * 18 + (col >> 5)) * 4) * 64 + ((col & 31) >> 3) * 16 + c15)) * 8 + (col & 4);
  u32x2 res[4];
#pragma unroll
  for (int m = 0; m < 4; m++) res[m] = *(const u32x2*)(xf + fo + m * 512);
  asm volatile("s_waitcnt vmcnt(22)" ::: "memory");
  bar();
  f32x4 acc[4];
#pragma unroll
  for (int m = 0; m < 4; m++) acc[m] = (f32x4){0, 0, 0, 0};
#pragma unroll
  for (int ks = 0; ks < 18; ks++) {
#pragma unroll
    for (int m = 0; m < 4; m++) {
      u32x4 a = *(const u32x4*)(sA + (ks * 4 + m) * 1024 + l * 16);
      acc[m] = mfma16(bw[ks], a, acc[m]);
    }
  }
  if (valid) {
#pragma unroll
    for (int m = 0; m < 4; m++) {
      u32x2 o = {pk2(acc[m][0] + blo(res[m][0]), acc[m][1] + bhi(res[m][0])),
                 pk2(acc[m][2] + blo(res[m][1]), acc[m][3] + bhi(res[m][1]))};
      *(u32x2*)(x1 + fo + m * 512) = o;
    }
  }
}

constexpr int RD = 5;
__global__ __launch_bounds__(768) void f_k(const u16* __restrict__ x1, const u16* __restrict__ WF,
                                            const u16* __restrict__ b1, u16* __restrict__ P) {
  __shared__ __attribute__((aligned(16))) char sA[TILE_B];
  __shared__ __attribute__((aligned(16))) char sH[TILE_B];
  __shared__ f32x2 st[3 * 64];
  const int tid = threadIdx.x, w = __builtin_amdgcn_readfirstlane(tid >> 6), l = tid & 63;
  const int c = blockIdx.x & 7, rt = blockIdx.x >> 3;
  const int c15 = l & 15, g = l >> 4;
  const int mw = w & 3, th = w >> 2;
  u32x4 xr[6];
  {
    const char* src = (const char*)x1 + (size_t)rt * TILE_B + (size_t)(th * 24 + mw) * 1024 + l * 16;
#pragma unroll
    for (int i = 0; i < 6; i++) xr[i] = *(const u32x4*)(src + i * 4096);
  }
  u32x2 bias[3];
#pragma unroll
  for (int i = 0; i < 3; i++) bias[i] = *(const u32x2*)(b1 + c * 576 + (3 * w + i) * 16 + 4 * g);
  const char* wp = (const char*)WF + (size_t)c * 36 * 36864 + w * 3072 + l * 16;
  u32x4 ring[RD][3];
#pragma unroll
  for (int s = 0; s < RD; s++)
#pragma unroll
    for (int i = 0; i < 3; i++) ring[s][i] = *(const u32x4*)(wp + s * 36864 + i * 1024);
  __builtin_amdgcn_sched_barrier(0);
  {
    float s = 0.f;
#pragma unroll
    for (int i = 0; i < 6; i++)
#pragma unroll
      for (int q = 0; q < 4; q++) s += blo(xr[i][q]) + bhi(xr[i][q]);
    float mu = s * (1.f / 48.f), m2 = 0.f;
#pragma unroll
    for (int i = 0; i < 6; i++)
#pragma unroll
      for (int q = 0; q < 4; q++) {
        float d0 = blo(xr[i][q]) - mu, d1 = bhi(xr[i][q]) - mu;
        m2 += d0 * d0 + d1 * d1;
      }
    float cnt = 48.f;
#pragma unroll
    for (int o = 16; o <= 32; o <<= 1) {
      float mu2 = __shfl_xor(mu, o), m22 = __shfl_xor(m2, o);
      float d = mu - mu2;
      m2 = m2 + m22 + d * d * (cnt * 0.5f);
      mu = 0.5f * (mu + mu2);
      cnt *= 2.f;
    }
    if (l < 16) { f32x2 t = {mu, m2}; st[th * 64 + mw * 16 + l] = t; }
    bar();
    f32x2 t0 = st[mw * 16 + c15], t1 = st[64 + mw * 16 + c15], t2 = st[128 + mw * 16 + c15];
    float mean = (t0[0] + t1[0] + t2[0]) * (1.f / 3.f);
    float e0 = t0[0] - mean, e1 = t1[0] - mean, e2 = t2[0] - mean;
    float M2 = t0[1] + t1[1] + t2[1] + 192.f * (e0 * e0 + e1 * e1 + e2 * e2);
    float rstd = 1.0f / sqrtf(M2 * (1.f / 576.f) + 1e-5f);
#pragma unroll
    for (int i = 0; i < 6; i++) {
      u32x4 o;
#pragma unroll
      for (int q = 0; q < 4; q++) o[q] = pk2((blo(xr[i][q]) - mean) * rstd, (bhi(xr[i][q]) - mean) * rstd);
      *(u32x4*)(sA + (th * 24 + mw + i * 4) * 1024 + l * 16) = o;
    }
    bar();
  }
  f32x4 acc[3][4];
#pragma unroll
  for (int i = 0; i < 3; i++)
#pragma unroll
    for (int m = 0; m < 4; m++) acc[i][m] = (f32x4){0, 0, 0, 0};
#pragma unroll
  for (int s = 0; s < 36; s++) {
    if (s == 18) {
#pragma unroll
      for (int i = 0; i < 3; i++) {
        const int nt = 3 * w + i;
        char* dst = sH + ((nt >> 1) * 4 * 64 + ((nt & 1) * 2 + (g >> 1)) * 16 + c15) * 16 + (g & 1) * 8;
        const float b0 = blo(bias[i][0]), b1v = bhi(bias[i][0]), b2v = blo(bias[i][1]), b3 = bhi(bias[i][1]);
#pragma unroll
        for (int m = 0; m < 4; m++) {
          float h0 = fmaxf(acc[i][m][0] + b0, 0.f), h1 = fmaxf(acc[i][m][1] + b1v, 0.f);
          float h2 = fmaxf(acc[i][m][2] + b2v, 0.f), h3 = fmaxf(acc[i][m][3] + b3, 0.f);
          u32x2 o = {pk2(h0 * h0, h1 * h1), pk2(h2 * h2, h3 * h3)};
          *(u32x2*)(dst + m * 1024) = o;
          acc[i][m] = (f32x4){0, 0, 0, 0};
        }
      }
      bar();
    }
    const char* ab = (s < 18 ? sA : sH) + (s % 18) * 4096 + l * 16;
    u32x4 a[4];
#pragma unroll
    for (int m = 0; m < 4; m++) a[m] = *(const u32x4*)(ab + m * 1024);
#pragma unroll
    for (int i = 0; i < 3; i++)
#pragma unroll
      for (int m = 0; m < 4; m++) acc[i][m] = mfma16(ring[s % RD][i], a[m], acc[i][m]);
    if (s + RD < 36) {
#pragma unroll
      for (int i = 0; i < 3; i++) ring[s % RD][i] = *(const u32x4*)(wp + (s + RD) * 36864 + i * 1024);
    }
    __builtin_amdgcn_sched_barrier(0);
  }
  char* pb = (char*)P + (size_t)c * ACT_B + (size_t)rt * TILE_B;
#pragma unroll
  for (int i = 0; i < 3; i++) {
    const int nt = 3 * w + i;
    char* dst = pb + ((nt >> 1) * 4 * 64 + ((nt & 1) * 2 + (g >> 1)) * 16 + c15) * 16 + (g & 1) * 8;
#pragma unroll
    for (int m = 0; m < 4; m++) {
      u32x2 o = {pk2(acc[i][m][0], acc[i][m][1]), pk2(acc[i][m][2], acc[i][m][3])};
      *(u32x2*)(dst + m * 1024) = o;
    }
  }
}

extern "C" void kernel_launch(void* const* d_in, const int* in_sizes, int n_in, void* d_out, int out_size,
                              void* d_ws, size_t ws_size, hipStream_t stream) {
  const u16* x0 = (const u16*)d_in[0];
  const u16* wqkv = (const u16*)d_in[1];
  const u16* wphi = (const u16*)d_in[2];
  const u16* wo = (const u16*)d_in[3];
  const u16* w1 = (const u16*)d_in[5];
  const u16* b1 = (const u16*)d_in[6];
  const u16* w2 = (const u16*)d_in[7];
  const u16* b2 = (const u16*)d_in[8];
  char* ws = (char*)d_ws;
  size_t off = 0;
  auto alloc = [&](size_t b) { void* p = ws + off; off += (b + 4095) & ~(size_t)4095; return p; };
  u16* WA = (u16*)alloc((size_t)8 * 18 * 14 * 1024);
  u16* WO = (u16*)alloc((size_t)8 * 18 * 5 * 1024);
  u16* WF = (u16*)alloc((size_t)8 * 36 * 36 * 1024);
  u16 *xf[2], *xn[2], *pqT[2], *of[2], *x1[2], *P[2];
  float* kvp[2];
  for (int i = 0; i < 2; i++) {
    xf[i] = (u16*)alloc(ACT_B); xn[i] = (u16*)alloc(ACT_B); pqT[i] = (u16*)alloc(ACT_B);
    of[i] = (u16*)alloc(ACT_B); x1[i] = (u16*)alloc(ACT_B); P[i] = (u16*)alloc(8 * ACT_B);
    kvp[i] = (float*)alloc((size_t)32 * 32 * 720 * 4);
  }
  prep_k<<<PREP_BLOCKS, 576, 0, stream>>>(x0, wqkv, wphi, wo, w1, w2, xf[0], xn[0], WA, WO, WF);
  for (int i = 0; i < 2; i++) {
    k1_k<<<256, 448, 0, stream>>>(xn[i], WA, pqT[i], kvp[i]);
    kr_k<<<256, 256, 0, stream>>>(pqT[i], kvp[i], of[i]);
    k2_k<<<256, 320, 0, stream>>>(of[i], WO, xf[i], x1[i]);
    f_k<<<256, 768, 0, stream>>>(x1[i], WF, b1, P[i]);
    if (i == 0) red_k<1><<<256, 576, 0, stream>>>(x1[i], P[i], b2, xf[1], xn[1], nullptr);
    else red_k<2><<<256, 576, 0, stream>>>(x1[i], P[i], b2, nullptr, nullptr, (u16*)d_out);
  }
}
```

```cpp
#define RXZ 0
#include <hip/hip_runtime.h>
#include <stdint.h>

typedef unsigned short u16;
typedef __attribute__((ext_vector_type(4))) unsigned u32x4;
typedef __attribute__((ext_vector_type(2))) unsigned u32x2;
typedef __attribute__((ext_vector_type(4))) float f32x4;
typedef __attribute__((ext_vector_type(2))) float f32x2;
typedef __attribute__((ext_vector_type(8))) __bf16 bf16x8;
typedef __attribute__((ext_vector_type(2))) __bf16 bf16x2;
typedef __attribute__((address_space(1))) unsigned gu32;
#define RLXA __ATOMIC_RELAXED, __HIP_MEMORY_SCOPE_AGENT
#define RLXW __ATOMIC_RELAXED, __HIP_MEMORY_SCOPE_WORKGROUP

constexpr int S_ = 2048, D_ = 576, F_ = 4608, N3 = 1728;
constexpr int TILE_B = 73728;
constexpr int ACT_B = S_ * D_ * 2;

__device__ __forceinline__ unsigned pk2(float a, float b) {
  f32x2 v = {a, b};
  bf16x2 r = __builtin_convertvector(v, bf16x2);
  return __builtin_bit_cast(unsigned, r);
}
__device__ __forceinline__ float blo(unsigned u) { return __uint_as_float(u << 16); }
__device__ __forceinline__ float bhi(unsigned u) { return __uint_as_float(u & 0xffff0000u); }
constexpr int WF_CH = 37 * 36864;
__device__ __forceinline__ void split3(float x, unsigned& hi, unsigned& mid, unsigned& lo) {
  hi = pk2(x, 0.f) & 0xffffu;
  const float r = x - __uint_as_float(hi << 16);
  mid = pk2(r, 0.f) & 0xffffu;
  lo = pk2(r - __uint_as_float(mid << 16), 0.f) & 0xffffu;
}
__device__ __forceinline__ f32x4 mfma16(u32x4 a, u32x4 b, f32x4 c) {
  return __builtin_amdgcn_mfma_f32_16x16x32_bf16(__builtin_bit_cast(bf16x8, a), __builtin_bit_cast(bf16x8, b), c, 0, 0, 0);
}
__device__ __forceinline__ void glds16(const char* sbase, unsigned voff, unsigned lds) {
  asm volatile("s_mov_b32 m0, %0\n\ts_nop 0\n\tglobal_load_lds_dwordx4 %1, %2 sc1" ::"s"(lds), "v"(voff), "s"(sbase) : "memory");
}
__device__ __forceinline__ void setprio(int p) {
  if (p <= 0) __builtin_amdgcn_s_setprio(0);
  else if (p == 1) __builtin_amdgcn_s_setprio(1);
  else if (p == 2) __builtin_amdgcn_s_setprio(2);
  else __builtin_amdgcn_s_setprio(3);
}
__device__ __forceinline__ void bar() {
  asm volatile("s_waitcnt lgkmcnt(0)" ::: "memory");
  __builtin_amdgcn_s_barrier();
  asm volatile("" ::: "memory");
}
#define VMW(N) asm volatile("s_waitcnt vmcnt(%0)" ::"n"(N) : "memory")

__device__ __forceinline__ u32x4 pair16(unsigned x0, unsigned x1, unsigned y0, unsigned y1) {
  u32x2 a = __builtin_amdgcn_permlane16_swap(x0, y0, false, false);
  u32x2 b = __builtin_amdgcn_permlane16_swap(x1, y1, false, false);
  return (u32x4){a[0], b[0], a[1], b[1]};
}
__device__ __forceinline__ void st16_wt(void* p, u32x4 v) {
  asm volatile("global_store_dwordx4 %0, %1, off sc1\n\ts_nop 1" ::"v"(p), "v"(v) : "memory");
}
__device__ __forceinline__ void st16s(char* sbase, unsigned voff, u32x4 v) {
  asm volatile("global_store_dwordx4 %0, %1, %2 sc1\n\ts_nop 1" ::"v"(voff), "v"(v), "s"(sbase) : "memory");
}
__device__ __forceinline__ void st8s(char* sbase, unsigned voff, u32x2 v) {
  asm volatile("global_store_dwordx2 %0, %1, %2 sc1\n\ts_nop 1" ::"v"(voff), "v"(v), "s"(sbase) : "memory");
}
__device__ __forceinline__ void st4s(char* sbase, unsigned voff, unsigned v) {
  asm volatile("global_store_dword %0, %1, %2 sc1\n\ts_nop 1" ::"v"(voff), "v"(v), "s"(sbase) : "memory");
}
__device__ __forceinline__ float xsum(float x) {
  u32x2 a = __builtin_amdgcn_permlane16_swap(__float_as_uint(x), __float_as_uint(x), false, false);
  x = __uint_as_float(a[0]) + __uint_as_float(a[1]);
  u32x2 b = __builtin_amdgcn_permlane32_swap(__float_as_uint(x), __float_as_uint(x), false, false);
  return __uint_as_float(b[0]) + __uint_as_float(b[1]);
}
template <int C>
__device__ __forceinline__ float dppf(float x) {
  return __int_as_float(__builtin_amdgcn_update_dpp(0, __float_as_int(x), C, 0xf, 0xf, false));
}
__device__ __forceinline__ float sum32(float x) {
  x += dppf<0xB1>(x);
  x += dppf<0x4E>(x);
  x += dppf<0x141>(x);
  x += dppf<0x140>(x);
  u32x2 a = __builtin_amdgcn_permlane16_swap(__float_as_uint(x), __float_as_uint(x), false, false);
  return __uint_as_float(a[0]) + __uint_as_float(a[1]);
}

#define ST(i)
#define STW(i, wv)
#define STP(i)
#define TM(i)
#define STC(i, wv)

constexpr int SL_B = 9216;
constexpr int X1S_B = 12288;
constexpr int O_XF = 0, O_XN = O_XF + ACT_B, O_OF = O_XN + ACT_B, O_X1 = O_OF + ACT_B;
constexpr int O_KVP = O_X1 + 256 * X1S_B;
constexpr int KVP_B = 8 * 23 * 16384;
constexpr int O_KVS = O_KVP + KVP_B;
#define NC 1
constexpr int O_PP = O_KVS + 8 * NC * 6144;
constexpr int LAY_B = O_PP + 8 * ACT_B;
#define PRV 1
constexpr int FC = PRV ? 8 : 1, FH_ = 8192 * FC;
constexpr int FL_N = 2 * 4 * 32 * 8 * 32 * FC;
constexpr size_t O_PQ = 2 * (size_t)LAY_B;
constexpr unsigned SENT = 0xffffffffu;
#define SPIN_CAP (1u << 18)

__device__ __forceinline__ void ln_part(const f32x2* v, f32x2* st, int w, int l) {
  const f32x2 s = (v[0] + v[1]) + (v[2] + v[3]);
  float mu = (s[0] + s[1]) * 0.125f;
  f32x2 q = {0.f, 0.f};
#pragma unroll
  for (int i = 0; i < 4; i++) { const f32x2 d = v[i] - (f32x2){mu, mu}; q = i ? q + d * d : d * d; }
  float m2 = q[0] + q[1];
  {
    const float mu2 = dppf<0x128>(mu), m22 = dppf<0x128>(m2), d = mu - mu2;
    m2 = m2 + m22 + d * d * 4.f;
    mu = 0.5f * (mu + mu2);
  }
  {
    u32x2 a = __builtin_amdgcn_permlane16_swap(__float_as_uint(mu), __float_as_uint(mu), false, false);
    u32x2 b = __builtin_amdgcn_permlane16_swap(__float_as_uint(m2), __float_as_uint(m2), false, false);
    const float d = __uint_as_float(a[0]) - __uint_as_float(a[1]);
    m2 = __uint_as_float(b[0]) + __uint_as_float(b[1]) + d * d * 8.f;
    mu = 0.5f * (__uint_as_float(a[0]) + __uint_as_float(a[1]));
  }
  {
    u32x2 a = __builtin_amdgcn_permlane32_swap(__float_as_uint(mu), __float_as_uint(mu), false, false);
    u32x2 b = __builtin_amdgcn_permlane32_swap(__float_as_uint(m2), __float_as_uint(m2), false, false);
    const float d = __uint_as_float(a[0]) - __uint_as_float(a[1]);
    m2 = __uint_as_float(b[0]) + __uint_as_float(b[1]) + d * d * 16.f;
    mu = 0.5f * (__uint_as_float(a[0]) + __uint_as_float(a[1]));
  }
  if (l < 8) { f32x2 t = {mu, m2}; st[w * 8 + l] = t; }
}
__device__ __forceinline__ u32x4 ln_fin(const f32x2* v, const f32x2* st, int r) {
  f32x2 t[9];
#pragma unroll
  for (int i = 0; i < 9; i++) t[i] = st[i * 8 + r];
  const f32x2 s = (((t[0] + t[1]) + (t[2] + t[3])) + ((t[4] + t[5]) + (t[6] + t[7]))) + t[8];
  const float mean = s[0] * (1.f / 9.f);
  float e = 0.f;
#pragma unroll
  for (int i = 0; i < 9; i++) { const float d = t[i][0] - mean; e += d * d; }
  const float rstd = __builtin_amdgcn_rsqf((s[1] + 64.f * e) * (1.f / 576.f) + 1e-5f);
  u32x4 o;
#pragma unroll
  for (int i = 0; i < 4; i++) { const f32x2 y = (v[i] - (f32x2){mean, mean}) * (f32x2){rstd, rstd}; o[i] = pk2(y[0], y[1]); }
  return o;
}

constexpr int PK_STR = 68;
constexpr int VT_STR = 72;
constexpr int RC = 0, R0 = 12288, RA = R0 + 4096, RB = RA + TILE_B, LDS_B = RB + TILE_B;
constexpr int PK_H = 6176;
constexpr int L_PK = RB, L_V = L_PK + 4 * PK_H, L_PQ = L_V + 73 * VT_STR * 2, L_PK2 = L_PQ + 72 * PK_STR * 4;
constexpr int L_PHQ = RA, L_TR = RA + 4 * 64 * VT_STR * 2;
static_assert(L_PK2 + 64 * PK_STR * 4 <= LDS_B && L_TR + 8 * 4096 <= RB, "LDS map");
constexpr int L_CNT = RC, L_R2 = RC + 256, L_KV = RC + 512;
enum { C_X0 = 0, C_K1 = 4, C_SM, C_PQ, C_PHQ, C_KV, C_OPUB, C_O, C_X1PUB, C_X1, C_WFP, C_WF, C_F0, C_F1, C_F2, C_F3, C_PPUB, C_P, C_LN, C_XNPUB, C_S2 };

__device__ __forceinline__ void lsig_(unsigned a, unsigned v) {
  unsigned t0, t1;
  unsigned long long ex;
  asm volatile("s_waitcnt lgkmcnt(0)\n\ts_mov_b64 %2, exec\n\ts_mov_b64 exec, 1\n\tv_mov_b32 %0, %3\n\tv_mov_b32 %1, %4\n\t"
               "ds_add_u32 %0, %1\n\ts_mov_b64 exec, %2" : "=&v"(t0), "=&v"(t1), "=&s"(ex) : "s"(a), "n"(v) : "memory");
}
#define lsig(C) lsig_(lds0 + L_CNT + 4 * (C), 1u)
#define LSLP 1
template <int SLP>
__device__ __forceinline__ void lwait_(unsigned a, unsigned t) {
  unsigned t0, t1;
  asm volatile("v_mov_b32 %0, %2\n"
               "1:\n\tds_read_b32 %1, %0\n\ts_waitcnt lgkmcnt(0)\n\tv_cmp_gt_u32 vcc, %3, %1\n\ts_cbranch_vccz 2f\n\t"
               "s_sleep %4\n\ts_branch 1b\n2:" : "=&v"(t0), "=&v"(t1) : "s"(a), "s"(t), "n"(SLP) : "vcc", "memory");
}
#define lwait(C, T) lwait_<LSLP>(lds0 + L_CNT + 4 * (C), T)
template <unsigned C>
__device__ __forceinline__ unsigned vconst() { unsigned z; asm volatile("v_mov_b32 %0, %1" : "=v"(z) : "n"(C)); return z; }
__device__ __forceinline__ void publish_(unsigned a, unsigned n, unsigned* f0, unsigned* f1) {
  unsigned t0, t1;
  unsigned long long ex;
  asm volatile("s_mov_b64 %2, exec\n\ts_mov_b64 exec, 1\n\tv_mov_b32 %0, %3\n\tv_mov_b32 %1, 1\n\t"
               "ds_add_rtn_u32 %0, %0, %1\n\ts_waitcnt lgkmcnt(0)\n\tv_cmp_eq_u32 vcc, %4, %0\n\ts_cbranch_vccz 1f\n\t"
               "v_mov_b32 %0, 0\n\tglobal_store_dword %0, %1, %5 sc1\n\tglobal_store_dword %0, %1, %6 sc1\n\ts_nop 1\n"
               "1:\n\ts_mov_b64 exec, %2"
               : "=&v"(t0), "=&v"(t1), "=&s"(ex) : "s"(a), "s"(n - 1), "s"(f0), "s"(f1) : "vcc", "memory");
}
__device__ __forceinline__ void publish_(unsigned a, unsigned n, unsigned* f0) {
  unsigned t0, t1;
  unsigned long long ex;
  asm volatile("s_mov_b64 %2, exec\n\ts_mov_b64 exec, 1\n\tv_mov_b32 %0, %3\n\tv_mov_b32 %1, 1\n\t"
               "ds_add_rtn_u32 %0, %0, %1\n\ts_waitcnt lgkmcnt(0)\n\tv_cmp_eq_u32 vcc, %4, %0\n\ts_cbranch_vccz 1f\n\t"
               "v_mov_b32 %0, 0\n\tglobal_store_dword %0, %1, %5 sc1\n\ts_nop 1\n"
               "1:\n\ts_mov_b64 exec, %2"
               : "=&v"(t0), "=&v"(t1), "=&s"(ex) : "s"(a), "s"(n - 1), "s"(f0) : "vcc", "memory");
}
#define publish(C, ...) publish_(lds0 + L_CNT + 4 * (C), __VA_ARGS__)
__device__ __forceinline__ void publish8_(unsigned a, unsigned n, unsigned* f0) {
  if (!PRV) { publish_(a, n, f0); return; }
  unsigned t0, t1;
  unsigned long long ex;
  asm volatile("s_mov_b64 %2, exec\n\ts_mov_b64 exec, 1\n\tv_mov_b32 %0, %3\n\tv_mov_b32 %1, 1\n\t"
               "ds_add_rtn_u32 %0, %0, %1\n\ts_waitcnt lgkmcnt(0)\n\tv_cmp_eq_u32 vcc, %4, %0\n\ts_cbranch_vccz 1f\n\t"
               "s_mov_b64 exec, 0xff\n\tv_mbcnt_lo_u32_b32 %0, -1, 0\n\tv_mov_b32 %1, 1\n\tv_lshlrev_b32 %0, 10, %0\n\t"
               "global_store_dword %0, %1, %5 sc1\n\ts_nop 1\n"
               "1:\n\ts_mov_b64 exec, %2"
               : "=&v"(t0), "=&v"(t1), "=&s"(ex) : "s"(a), "s"(n - 1), "s"(f0) : "vcc", "memory");
}
#define publish8(C, ...) publish8_(lds0 + L_CNT + 4 * (C), __VA_ARGS__)
#define POLS 1
__device__ __forceinline__ void poll(const unsigned* f, unsigned i) {
  unsigned t0, n;
  asm volatile("s_mov_b32 %1, %4\n"
               "1:\n\tglobal_load_dword %0, %2, %3 sc1\n\ts_waitcnt vmcnt(0)\n\tv_cmp_eq_u32 vcc, 0, %0\n\ts_cbranch_vccz 2f\n\t"
               "s_sub_u32 %1, %1, 1\n\ts_cbranch_scc1 2f\n\ts_sleep %5\n\ts_branch 1b\n2:"
               : "=&v"(t0), "=&s"(n) : "v"(i * 4), "s"(f), "n"(SPIN_CAP), "n"(POLS) : "vcc", "scc", "memory");
}
#define PP2 0
__device__ __forceinline__ void poll2(const unsigned* f, unsigned i, unsigned& t0, unsigned& t1) {
  if (!PP2) { poll(f, i); return; }
  unsigned n;
  asm volatile("s_mov_b32 %2, %5\n\tglobal_load_dword %0, %3, %4 sc1\n\ts_sleep %6\n"
               "1:\n\tglobal_load_dword %1, %3, %4 sc1\n\ts_waitcnt vmcnt(1)\n\tv_cmp_eq_u32 vcc, 0, %0\n\ts_cbranch_vccz 2f\n\t"
               "global_load_dword %0, %3, %4 sc1\n\ts_waitcnt vmcnt(1)\n\tv_cmp_eq_u32 vcc, 0, %1\n\ts_cbranch_vccz 2f\n\t"
               "s_sub_u32 %2, %2, 1\n\ts_cbranch_scc0 1b\n2:"
               : "=&v"(t0), "=&v"(t1), "=&s"(n) : "v"(i * 4), "s"(f), "n"(SPIN_CAP), "n"(PP2) : "vcc", "scc", "memory");
}
#define KEEP asm volatile("" :: "v"(t0), "v"(t1));
__device__ __forceinline__ unsigned zero() { unsigned z; asm volatile("v_mov_b32 %0, 0" : "=v"(z)); return z; }
__device__ __forceinline__ unsigned umax4(u32x4 v) { return max(max(v[0], v[1]), max(v[2], v[3])); }

#define FV 0
__device__ __forceinline__ void feat18(const float* src, float sgn, unsigned* q) {
  const float c = sgn * 1.44269504088896f;
  f32x2 t[9];
#pragma unroll
  for (int f = 0; f < 9; f++) t[f] = (f32x2){src[2 * f], src[2 * f + 1]} * (f32x2){c, c};
  float mx = fmaxf(t[0][0], t[0][1]);
#pragma unroll
  for (int f = 1; f < 9; f++) mx = fmaxf(fmaxf(mx, t[f][0]), t[f][1]);
  f32x2 s = {0.f, 0.f};
#pragma unroll
  for (int f = 0; f < 9; f++) {
    t[f] -= (f32x2){mx, mx};
    t[f] = (f32x2){__builtin_amdgcn_exp2f(t[f][0]), __builtin_amdgcn_exp2f(t[f][1])};
    s = f ? s + t[f] : t[f];
  }
  const float r = __builtin_amdgcn_rcpf(s[0] + s[1]);
#pragma unroll
  for (int f = 0; f < 9; f++) { t[f] *= (f32x2){r, r}; q[f] = pk2(t[f][0], t[f][1]); }
}
__device__ __forceinline__ float sfma(float a, float b, float c) { float r; asm("v_fma_f32 %0, %1, %2, %3" : "=v"(r) : "v"(a), "v"(b), "v"(c)); return r; }
__device__ __forceinline__ float sadd(float a, float b) { float r; asm("v_add_f32 %0, %1, %2" : "=v"(r) : "v"(a), "v"(b)); return r; }
__device__ __forceinline__ float ssub(float a, float b) { float r; asm("v_sub_f32 %0, %1, %2" : "=v"(r) : "v"(a), "v"(b)); return r; }
__device__ __forceinline__ float smul(float a, float b) { float r; asm("v_mul_f32 %0, %1, %2" : "=v"(r) : "v"(a), "v"(b)); return r; }
__device__ __forceinline__ void feat36(const float* p, unsigned* q) {
  float mx = fmaxf(p[0], p[1]), mn = fminf(p[0], p[1]);
#pragma unroll
  for (int f = 2; f < 18; f += 2) { mx = fmaxf(fmaxf(mx, p[f]), p[f + 1]); mn = fminf(fminf(mn, p[f]), p[f + 1]); }
  float a[18], b[18];
#pragma unroll
  for (int f = 0; f < 18; f++) { a[f] = __builtin_amdgcn_exp2f(ssub(p[f], mx)); b[f] = __builtin_amdgcn_exp2f(ssub(mn, p[f])); }
  float sa[2] = {a[0], a[1]}, sb[2] = {b[0], b[1]};
#pragma unroll
  for (int f = 2; f < 18; f++) { sa[f & 1] = sadd(sa[f & 1], a[f]); sb[f & 1] = sadd(sb[f & 1], b[f]); }
  const float ra = __builtin_amdgcn_rcpf(sa[0] + sa[1]), rb = __builtin_amdgcn_rcpf(sb[0] + sb[1]);
#pragma unroll
  for (int f = 0; f < 9; f++) {
    q[f] = pk2(smul(a[2 * f], ra), smul(a[2 * f + 1], ra));
    q[9 + f] = pk2(smul(b[2 * f], rb), smul(b[2 * f + 1], rb));
  }
}

#define RD 5
#define PRE 2
#define FPRI 3
#define FP2 25
#define FP3 31
#define WNA 0
#define WNB 12
#define WNC 0
#define NAC 1
#define CT 0
#define CTW 0
#define VDL 8
#define QDL 3
#define PKS 3
#define VR 9
#define DRY 0
#define PKPRI 3
#define VPRI 2
#define WAUX 16
#define PSLP 0
#define FSLP 2
constexpr int FE = R0, FA = RB, FH = RA;

__device__ __forceinline__ int pick(unsigned a, unsigned t, unsigned& done, unsigned& c) {
  int m;
  asm volatile("1:\n\tv_cmp_le_u32 vcc, %4, %0\n\ts_andn2_b32 %1, vcc_lo, %2\n\ts_cbranch_scc1 2f\n\t"
               "ds_read_b32 %0, %3\n\ts_waitcnt lgkmcnt(0)\n\ts_branch 1b\n2:\n\ts_ff1_i32_b32 %1, %1\n\ts_bitset1_b32 %2, %1"
               : "+v"(c), "=&s"(m), "+s"(done) : "v"(a), "s"(t) : "vcc", "scc", "memory");
  done = __builtin_amdgcn_readfirstlane(done);
  m = __builtin_amdgcn_readfirstlane(m);
  __builtin_assume(m >= 0 && m < 4);
  return m;
}
#define MSLP 0
#define XR 9

typedef __attribute__((ext_vector_type(4))) short s16x4;
__device__ __forceinline__ u32x2 trrd(unsigned a) {
  return __builtin_bit_cast(u32x2, __builtin_amdgcn_ds_read_tr16_b64_v4i16((__attribute__((address_space(3))) s16x4*)a));
}
__device__ __forceinline__ u32x4 trrd2(unsigned a, unsigned b) { u32x2 x = trrd(a), y = trrd(b); return (u32x4){x[0], x[1], y[0], y[1]}; }
__device__ __forceinline__ void gldsp(const char* sbase, unsigned voff, unsigned lds) {
  asm volatile("s_mov_b32 m0, %0\n\ts_nop 0\n\tglobal_load_lds_dwordx4 %1, %2" ::"s"(lds), "v"(voff), "s"(sbase) : "memory");
}
__device__ __forceinline__ void gldsm(const char* sbase, unsigned voff, unsigned lds, unsigned long long mask) {
  asm volatile("s_mov_b32 m0, %0\n\ts_mov_b64 exec, %3\n\tglobal_load_lds_dwordx4 %1, %2\n\ts_mov_b64 exec, -1" ::"s"(lds), "v"(voff), "s"(sbase), "s"(mask) : "memory");
}
template <int C>
__device__ __forceinline__ unsigned dppu(unsigned x) { return (unsigned)__builtin_amdgcn_update_dpp(0, (int)x, C, 0xf, 0xf, false); }
__device__ __forceinline__ void tr4(const u32x2* r, u32x4* o, int l) {
#pragma unroll
  for (int p = 0; p < 4; p++) {
    o[0][p] = __builtin_amdgcn_perm(r[2 * p + 1][0], r[2 * p][0], 0x05040100u);
    o[1][p] = __builtin_amdgcn_perm(r[2 * p + 1][0], r[2 * p][0], 0x07060302u);
    o[2][p] = __builtin_amdgcn_perm(r[2 * p + 1][1], r[2 * p][1], 0x05040100u);
    o[3][p] = __builtin_amdgcn_perm(r[2 * p + 1][1], r[2 * p][1], 0x07060302u);
  }
  const bool b0 = l & 1, b1 = l & 2;
#pragma unroll
  for (int i = 0; i < 4; i += 2)
#pragma unroll
    for (int p = 0; p < 4; p++) {
      const unsigned x = o[i][p], y = o[i + 1][p], xs = dppu<0xB1>(x), ys = dppu<0xB1>(y);
      o[i][p] = b0 ? ys : x; o[i + 1][p] = b0 ? y : xs;
    }
#pragma unroll
  for (int i = 0; i < 2; i++)
#pragma unroll
    for (int p = 0; p < 4; p++) {
      const unsigned x = o[i][p], y = o[i + 2][p], xs = dppu<0x4E>(x), ys = dppu<0x4E>(y);
      o[i][p] = b1 ? ys : x; o[i + 2][p] = b1 ? y : xs;
    }
}
constexpr int WO_X = 8 * 18 * 4, T_WO = 168;
#define HSLP 4
#define LAG 1
#define CPN 32
constexpr int K_X = 0, K_R = TILE_B, K_SL = 13824, K_M = K_R + 6 * K_SL;
constexpr int K_FULL = K_M, K_FREE = K_M + 128, K_WFL = K_M + 224, K_ISS = K_M + 248, K_CNT = K_M + 252, K_CS = K_M + 256, K_WF = K_M + 1280, K_DUM = K_M + 5888;
static_assert(K_DUM + 1024 <= 163840, "LDS map");
#define SF(i, wv)
#define TS(i)
typedef const __attribute__((address_space(1))) u32x4* gp16;

extern "C" __global__ __launch_bounds__(768) void hh_first_k(const u16* __restrict__ x0, const u16* __restrict__ wqkv,
                                                             const u16* __restrict__ wphi, char* __restrict__ act,
                                                             char* __restrict__ WA, const u16* __restrict__ wo,
                                                             const u16* __restrict__ w1, const u16* __restrict__ w2,
                                                             char* __restrict__ WO, char* __restrict__ WF,
                                                             const u16* __restrict__ b1, unsigned* __restrict__ flg) {
  __shared__ __attribute__((aligned(16))) char sm[163840];
  const int w = __builtin_amdgcn_readfirstlane(threadIdx.x >> 6);
  __builtin_assume(w >= 0 && w < 12);
  const int xcd = blockIdx.x & 7, idx = blockIdx.x >> 3;
  const int g = (xcd & 1) * 4 + (idx >> 3), rt = (xcd >> 1) * 8 + (idx & 7), rot = rt % 9;
  const unsigned lds0 = (unsigned)(uintptr_t)(__attribute__((address_space(3))) char*)sm;
#define LV                                                                                           \
  int l;                                                                                             \
  asm volatile("v_mbcnt_lo_u32_b32 %0, -1, 0\n\tv_mbcnt_hi_u32_b32 %0, -1, %0" : "=v"(l));           \
  __builtin_assume(l >= 0 && l < 64);                                                                \
  const int c15 = l & 15, g4 = l >> 4;                                                               \
  (void)c15; (void)g4;
  u32x4 xr[9];
  const int wr = (w + 2 * (idx >> 3)) & 7;
  {
    LV
    {
      const char *c0, *c2;
      asm volatile("s_getpc_b64 s[20:21]\n\ts_add_u32 s20, s20, hh_first_k@rel32@lo+4\n\ts_addc_u32 s21, s21, hh_first_k@rel32@hi+12\n\t"
                   "s_mov_b64 %0, s[20:21]\n\t"
                   "s_getpc_b64 s[20:21]\n\ts_add_u32 s20, s20, hh_end_k@rel32@lo+4\n\ts_addc_u32 s21, s21, hh_end_k@rel32@hi+12\n\t"
                   "s_mov_b64 %1, s[20:21]" : "=&s"(c0), "=&s"(c2) :: "s20", "s21", "scc");
      const int cn = (int)(c2 - c0) - 4;
      asm volatile("s_mov_b32 m0, %0\n\ts_cmp_lt_u32 %3, 4\n\ts_cselect_b64 exec, -1, 0\n\ts_cmp_lt_u32 %4, %6\n\ts_cselect_b64 exec, exec, 0\n\t"
                   "s_cmp_gt_i32 %5, 0\n\ts_cselect_b64 exec, exec, 0\n\tglobal_load_lds_dword %1, %2\n\ts_mov_b64 exec, -1"
                   ::"s"(lds0 + K_DUM), "v"(min(((w & 3) * 64 + l) * 128, cn)), "s"(c0), "s"(w), "s"(blockIdx.x), "s"(cn), "n"(CPN) : "scc", "memory");
    }
    ((unsigned*)(sm + K_M))[l] = l >= 18 && l < 32 ? 4u : 0u;
    __builtin_amdgcn_sched_barrier(0);
    asm volatile("" ::"s"(x0), "s"(wqkv), "s"(wphi), "s"(act), "s"(WA), "s"(wo), "s"(w1), "s"(w2), "s"(WO), "s"(WF), "s"(b1), "s"(flg));
    bar();
    const char* xp = (const char*)x0 + (size_t)(rt * 64 + wr * 8) * 1152;
    const unsigned vo = (l >> 3) * 1152 + (l & 7) * 16;
    asm volatile("s_cmp_lt_u32 %11, 8\n\ts_cbranch_scc0 1f\n\t"
                 "global_load_dwordx4 %0, %9, %10\n\tglobal_load_dwordx4 %1, %9, %10 offset:128\n\tglobal_load_dwordx4 %2, %9, %10 offset:256\n\t"
                 "global_load_dwordx4 %3, %9, %10 offset:384\n\tglobal_load_dwordx4 %4, %9, %10 offset:512\n\tglobal_load_dwordx4 %5, %9, %10 offset:640\n\t"
                 "global_load_dwordx4 %6, %9, %10 offset:768\n\tglobal_load_dwordx4 %7, %9, %10 offset:896\n\tglobal_load_dwordx4 %8, %9, %10 offset:1024\n"
                 "1:"
                 : "=&v"(xr[0]), "=&v"(xr[1]), "=&v"(xr[2]), "=&v"(xr[3]), "=&v"(xr[4]), "=&v"(xr[5]), "=&v"(xr[6]), "=&v"(xr[7]), "=&v"(xr[8])
                 : "v"(vo), "s"(xp), "s"(w) : "scc", "memory");
  }
  const char* wb = (const char*)wqkv + g * 144;
  const int h = w & 3, par = h & 1;
#define VO_DEF                                                                                       \
  unsigned vo[7];                                                                                    \
  _Pragma("unroll") for (int k = 0; k < 7; k++) {                                                    \
    const unsigned id = min((h + 4 * k) * 64 + l, 1727), sg = (id * 7282u) >> 16, st = sg >= 96, im = (sg >> 5) - 3 * st; \
    vo[k] = (st * 32 + (sg & 24) + 4 * (sg & 1) + ((sg >> 1) & 3)) * 3456 + im * 1152 + (id - 9 * sg) * 16; \
    asm volatile("" : "+v"(vo[k]));                                                                  \
  }
#define PAIR_SRC(P) (wb + (size_t)((P) + rot >= 9 ? (P) + rot - 9 : (P) + rot) * 221184)
#define PAIR_PUT(P, R, ...)                                                                          \
  {                                                                                                  \
    char* d = sm + K_R + ((P) % 3) * 2 * K_SL + h * 1024 + l * 16;                                   \
    lwait_<1>(lds0 + K_FREE + 4 * (2 * (P) - 5), 4u);                                                \
    _Pragma("unroll") for (int k = 0; k < 4; k++) *(u32x4*)(d + k * 4096) = R[k];                    \
    lsig_(lds0 + K_FULL + 8 * (P), 1u);                                                              \
    _Pragma("unroll") for (int k = 4; k < 6; k++) *(u32x4*)(d + k * 4096) = R[k];                    \
    if (h < 3) *(u32x4*)(d + 6 * 4096) = R[6];                                                       \
    __VA_ARGS__                                                                                      \
    lsig_(lds0 + K_FULL + 8 * (P) + 4, 1u);                                                          \
  }
#define TA_DEF                                                                                       \
  unsigned ta[2];                                                                                    \
  ta[0] = lds0 + K_R + (8 * g4 + 2 * ((l >> 2) & 3)) * 144 + 32 * h + 8 * (h >> 1) + 8 * (l & 3);    \
  ta[1] = ta[0] + 32 - 8 * (l & 3) + ((l & 3) == 1 ? 9216 : (l & 3) == 2 ? 0 : 4608);
#define TA(i) ((i) == 0 ? ta[0] + 4608 : (i) == 1 ? ta[0] + 9216 : (i) == 2 ? ta[0] : ta[1])
#define WF_LD                                                                                        \
  wf[0] = *(const u32x4*)(sm + K_WF + h * 1152 + l * 16);                                            \
  wf[1] = *(const u32x4*)(sm + K_WF + h * 1152 + 1024 + g4 * 32 + (l & 1) * 16);
  if (__builtin_expect(w >= 8, 1)) {
    LV
    TS(0);
    VO_DEF
#define H_SIG(P, T) if ((P) - LAG < 3) { VMW(7 * LAG); lsig_(lds0 + K_FULL + ((P) < LAG ? 120 : 8 * ((P) - LAG)) + 4 * (T), 1u); }
    if (HSLP) __builtin_amdgcn_s_sleep(HSLP);
#pragma unroll
    for (int i = 0; i < 3; i++) {
      const char* sb = PAIR_SRC(i);
      const unsigned ld = lds0 + K_R + h * 1024 + i * 2 * K_SL;
#pragma unroll
      for (int k = 0; k < 4; k++) gldsp(sb, vo[k], ld + k * 4096);
      if (i >= LAG) H_SIG(i, 0)
#pragma unroll
      for (int k = 4; k < 7; k++) gldsp(sb, vo[k], k == 6 && h == 3 ? lds0 + K_DUM : ld + k * 4096);
      if (i >= LAG) H_SIG(i, 1)
    }
    TS(1);
    u32x4 r[4][7];
#pragma unroll
    for (int i = 0; i < 4; i++) {
      const char* sb = PAIR_SRC(5 + i);
#pragma unroll
      for (int k = 0; k < 7; k++) {
        r[i][k] = *(gp16)(sb + vo[k]);
        if (k == 3) H_SIG(3 + i, 0)
        if (k == 6) H_SIG(3 + i, 1)
      }
      asm volatile("" ::: "memory");
      if (i == 1) TS(2);
    }
    lsig_(lds0 + K_ISS, 1u);
    TS(3);
    PAIR_PUT(5, r[0])
    PAIR_PUT(6, r[1])
    TS(4);
    PAIR_PUT(7, r[2])
    PAIR_PUT(8, r[3], lwait_<1>(lds0 + K_WFL + 4 * h, 1u);)
    TS(5);
    if (h < 3) asm volatile("s_mov_b32 m0, %0\n\ts_nop 0\n\tglobal_load_lds_dword %1, %2" ::"s"(lds0 + K_DUM), "v"((blockIdx.x * 162 + min(h * 64 + l, 161)) * 128), "s"(w2) : "memory");
    if (rt < 23) {
#pragma unroll
      for (int i = 0; i < 4; i++) st16s(act + LAY_B + O_KVP + (g * 23 + rt) * 16384, h * 4096 + i * 1024 + l * 16, (u32x4){SENT, SENT, SENT, SENT});
    }
    if (rt < 18) {
      u32x4 wf[2];
      WF_LD
      TA_DEF
      const int jj = rt >= 9, ks = 2 * (rot >= 3 ? rot - 3 : rot + 6) + jj;
      lwait_<2>(lds0 + K_FULL + 4 * (12 + jj), 4u);
      u32x4 f[4];
#pragma unroll
      for (int i = 0; i < 4; i++) f[i] = trrd2(TA(i) + jj * K_SL, TA(i) + jj * K_SL + 144);
      f32x4 pp[2][2][2];
#pragma unroll
      for (int u = 0; u < 2; u++) {
        const unsigned iv = g4 == (c15 >> 2) ? ((c15 & 1) ? 0x3F800000u : 0x3F80u) : 0u;
        u32x4 id = {0, 0, 0, 0};
        id[2 * u] = (c15 & 2) ? 0u : iv; id[2 * u + 1] = (c15 & 2) ? iv : 0u;
        const f32x4 z = {0, 0, 0, 0};
        const f32x4 dk = mfma16(f[0], id, z), dq = mfma16(f[2], id, z), dx = mfma16(f[3], id, z);
        const unsigned x0_ = pk2(dx[0], dx[1]), x1_ = pk2(dx[2], dx[3]);
        const u32x4 ak = {pk2(dk[0], dk[1]), pk2(dk[2], dk[3]), x0_, x1_}, aq = {pk2(dq[0], dq[1]), pk2(dq[2], dq[3]), x0_, x1_};
#pragma unroll
        for (int t = 0; t < 2; t++) {
          pp[0][t][u] = mfma16(ak, (u32x4){wf[t][0], wf[t][1], g4 == 0 ? wf[t][2] : 0u, g4 == 0 ? wf[t][3] : 0u}, z);
          pp[1][t][u] = mfma16(aq, (u32x4){wf[t][0], wf[t][1], g4 == 2 ? wf[t][2] : 0u, g4 == 2 ? wf[t][3] : 0u}, z);
        }
      }
      char* wd = WA + (size_t)g * 18 * 14336;
#define WA_POS(vc) ((vc) < 208 ? ks * 14336 + ((vc) >> 4) * 1024 + g4 * 256 + ((vc) & 15) * 16 \
                               : (ks >> 1) * 14336 + 13312 + g4 * 256 + ((vc) - 208 + (ks & 1) * 8) * 16)
#pragma unroll
      for (int a = 0; a < 2; a++)
#pragma unroll
        for (int t = 0; t < 2; t++) {
          const int vc = a * 144 + 18 * h + t * 16 + c15;
          const u32x4 o = {pk2(pp[a][t][0][0], pp[a][t][0][1]), pk2(pp[a][t][0][2], pp[a][t][0][3]),
                           pk2(pp[a][t][1][0], pp[a][t][1][1]), pk2(pp[a][t][1][2], pp[a][t][1][3])};
          if (t == 0 || c15 < 2) st16s(wd, WA_POS(vc), o);
        }
      {
        const int v0 = 72 + 18 * h + c15 - 2 * par, v1 = v0 + 12;
        if (c15 >= 2 * par) st16s(wd, WA_POS(v0), f[1]);
        if (c15 >= 4 && c15 < 6 + 2 * par) st16s(wd, WA_POS(v1), f[3]);
      }
    }
    return;
  }
  {
    LV
    f32x2 v[36], s = {0.f, 0.f};
#pragma unroll
    for (int i = 0; i < 36; i++) {
      if ((i & 3) == 0) asm volatile("s_waitcnt vmcnt(%1)" : "+v"(xr[i >> 2]) : "n"(8 - (i >> 2)));
      v[i] = (f32x2){blo(xr[i >> 2][i & 3]), bhi(xr[i >> 2][i & 3])}; s += v[i];
    }
    SF(1, 0); SF(13, 7);
    float mu = s[0] + s[1];
    mu += dppf<0xB1>(mu); mu += dppf<0x4E>(mu); mu += dppf<0x141>(mu);
    mu *= (1.f / 576.f);
    f32x2 q = {0.f, 0.f};
#pragma unroll
    for (int i = 0; i < 36; i++) { v[i] -= (f32x2){mu, mu}; q += v[i] * v[i]; }
    float m2 = q[0] + q[1];
    m2 += dppf<0xB1>(m2); m2 += dppf<0x4E>(m2); m2 += dppf<0x141>(m2);
    const float rs = __builtin_amdgcn_rsqf(m2 * (1.f / 576.f) + 1e-5f);
    char* xd = sm + K_X + ((l >> 2) & 1) * 4096 + (wr >> 1) * 1024 + (l & 3) * 256 + ((((wr & 1) * 8 + (l >> 3)) ^ (l & 7)) * 16);
#pragma unroll
    for (int i = 0; i < 9; i++) {
      u32x4 o;
#pragma unroll
      for (int p = 0; p < 4; p++) { const f32x2 t = v[4 * i + p] * (f32x2){rs, rs}; o[p] = pk2(t[0], t[1]); }
      *(u32x4*)(xd + i * 8192) = o;
    }
    lsig_(lds0 + K_FULL, 1u);
    SF(2, 0); SF(14, 7);
  }
  if (w >= 4) {
    LV
    const int j = h, kq = g4, cq = ((l & 3) << 2) | ((l >> 2) & 3);
    u32x4 rw[2][7];
    {
      VO_DEF
#pragma unroll
      for (int i = 0; i < 2; i++) {
        const char* sb = PAIR_SRC(3 + i);
#pragma unroll
        for (int k = 0; k < 7; k++) rw[i][k] = *(gp16)(sb + vo[k]);
        asm volatile("" ::: "memory");
      }
    }
    unsigned we[2][8];
#pragma unroll
    for (int t = 0; t < 2; t++)
#pragma unroll
      for (int jj = 0; jj < 8; jj++) {
        const int d = (jj < 4 ? 4 * g4 + jj : 12 + jj) - 2 * par;
        we[t][jj] = *(const u16*)((const char*)(wphi + (g * 4 + j) * 324) + (unsigned)(min(max(d, 0), 17) * 18 + min(t * 16 + c15, 17)) * 2);
      }
    __builtin_amdgcn_sched_barrier(0);
    PAIR_PUT(3, rw[0])
    PAIR_PUT(4, rw[1])
    __builtin_amdgcn_sched_barrier(0);
    {
      u32x4 wf[2];
#pragma unroll
      for (int t = 0; t < 2; t++)
#pragma unroll
        for (int jj = 0; jj < 8; jj += 2) {
          const int d = (jj < 4 ? 4 * g4 + jj : 12 + jj) - 2 * par;
          wf[t][jj >> 1] = d >= 0 && d < 18 ? we[t][jj] | (we[t][jj + 1] << 16) : 0u;
        }
      *(u32x4*)(sm + K_WF + j * 1152 + l * 16) = wf[0];
      if (c15 < 2) *(u32x4*)(sm + K_WF + j * 1152 + 1024 + g4 * 32 + c15 * 16) = wf[1];
      lsig_(lds0 + K_WFL + 4 * j, 1u);
    }
    __builtin_amdgcn_sched_barrier(0);
    SF(23, 4);
    lwait_<2>(lds0 + K_ISS, 4u);
    SF(10, 4);
    const int cb = (idx * 43) >> 7, k3 = idx - 3 * cb;
    u32x2 r[2][8];
    char* dst[2];
#pragma unroll
    for (int i = 0; i < 2; i++) {
      const int sl = j + 4 * i;
      const char* sp;
      unsigned vo, sa, sb, sc;
      if (idx < 27) {
        const int ks = k3 * 6 + sl;
        sp = (const char*)(w1 + (size_t)ks * 32 * F_ + (xcd * 9 + cb) * 64);
        vo = (kq * 8 * F_ + 4 * cq) * 2; sa = 2 * F_; sb = 4 * F_; sc = 8 * F_;
        dst[i] = sl < 6 ? WF + (size_t)xcd * WF_CH + ks * 36864 + cb * 4096 : nullptr;
      } else {
        const int u = (idx - 27) * 5 + sl, t = min(u, 20) * 8 + xcd;
        const bool m = t < 144;
        const int cg = m ? t / 18 : (t - 144) / 3;
        const int ks = m ? t % 18 : min(2 * (((t - 144) % 3) * 4 + (cq >> 2)) + ((cq >> 1) & 1), 17), col = m ? 4 * cq : 64 + 4 * (cq & 1);
        const int ch = ks * 4 + kq, p = (ch * 57) >> 9, c = ch - 9 * p;
        sp = (const char*)(wo + cg * 72);
        vo = ((p * 72 + (c < 8 ? (c >> 1) * 18 + (c & 1) * 8 : 16)) * D_ + col) * 2;
        sa = 2 * D_; sb = c < 8 ? 4 * D_ : 36 * D_; sc = 2 * sb;
        dst[i] = sl < 5 && u < 21 ? WO + (size_t)t * 4096 : nullptr;
      }
      asm volatile("" : "+v"(vo), "+v"(sa), "+v"(sb), "+v"(sc), "+s"(sp));
      const __amdgpu_buffer_rsrc_t rs = __builtin_amdgcn_make_buffer_rsrc((void*)sp, 0, dst[i] ? 0x7fffffff : 0, 0x00020000);
#pragma unroll
      for (int e = 0; e < 8; e++)
        r[i][e] = __builtin_amdgcn_raw_buffer_load_b64(rs, vo + (e & 1) * sa + ((e >> 1) & 1) * sb + (e >> 2) * sc, 0, 0);
      __builtin_amdgcn_sched_barrier(0);
    }
    const unsigned bv = *(const u16*)((const char*)(b1 + (xcd * 9 + min(cb, 8)) * 64) + (unsigned)l * 2);
    __builtin_amdgcn_sched_barrier(0);
    SF(20, 4);
    __builtin_amdgcn_s_waitcnt(0x0F70);
    if (w == 7) {
      if (l < 8 * FC) st4s((char*)flg, (blockIdx.x * 8 * FC + l) * 128, 0u);
      if (l == 0) st4s((char*)flg, FL_N * 4 + blockIdx.x * 4, 0u);
#pragma unroll
      for (int i = 0; i < NC * 24; i += 64)
        if (i + l < NC * 24) st16s(act + (blockIdx.x >> 7) * LAY_B + O_KVS, (blockIdx.x & 127) * 384 * NC + (i + l) * 16, (u32x4){SENT, SENT, SENT, SENT});
      const char *c1, *c2;
      asm volatile("s_getpc_b64 s[20:21]\n\ts_add_u32 s20, s20, hh_mega_k@rel32@lo+4\n\ts_addc_u32 s21, s21, hh_mega_k@rel32@hi+12\n\t"
                   "s_mov_b64 %0, s[20:21]\n\t"
                   "s_getpc_b64 s[20:21]\n\ts_add_u32 s20, s20, hh_end_k@rel32@lo+4\n\ts_addc_u32 s21, s21, hh_end_k@rel32@hi+12\n\t"
                   "s_mov_b64 %1, s[20:21]" : "=&s"(c1), "=&s"(c2) :: "s20", "s21", "scc");
      const char* p = c1 + (size_t)(idx * 16 + c15) * 128;
      if (l < 16 && p < c2) asm volatile("s_mov_b32 m0, %0\n\ts_nop 0\n\tglobal_load_lds_dword %1, off" ::"s"(lds0 + K_DUM), "v"(p) : "memory");
    }
    f32x4 cs[4] = {{0, 0, 0, 0}, {0, 0, 0, 0}, {0, 0, 0, 0}, {0, 0, 0, 0}};
    const u32x4 one = {0x3F803F80u, 0x3F803F80u, 0x3F803F80u, 0x3F803F80u};
#pragma unroll
    for (int i = 0; i < 2; i++) {
      if (dst[i]) {
        u32x4 o[4];
        tr4(r[i], o, l);
#pragma unroll
        for (int f = 0; f < 4; f++) st16s(dst[i] + f * 1024, l * 16, o[f]);
#pragma unroll
        for (int f = 0; f < 4; f++) cs[f] = mfma16(one, o[f], cs[f]);
      }
    }
    SF(21, 4);
    if (idx < 27) {
      ((float*)(sm + K_CS))[j * 64 + l] = g4 == 0 ? cs[0][0] : g4 == 1 ? cs[1][0] : g4 == 2 ? cs[2][0] : cs[3][0];
      asm volatile("s_waitcnt lgkmcnt(0)" ::: "memory");
      unsigned n = 0;
      if (l == 0) n = __hip_atomic_fetch_add((unsigned*)(sm + K_CNT), 1u, RLXW);
      asm volatile("" ::: "memory");
      if (__builtin_amdgcn_readfirstlane(n) == 3u) {
        const float* cp = (const float*)(sm + K_CS) + l;
        unsigned hi, m, lo;
        split3((cp[0] + cp[64]) + (cp[128] + cp[192]), hi, m, lo);
        const u32x4 o = {hi | (m << 16), hi | (m << 16), lo | (hi << 16), k3 ? 0u : bv | (bv << 16)};
        char* d = WF + (size_t)xcd * WF_CH + 18 * 36864 + cb * 4096;
        st16s(d, g4 * 1024 + c15 * 16 + k3 * 256, o);
        const unsigned z = zero();
        if (k3 == 0) st16s(d, g4 * 1024 + c15 * 16 + 768, (u32x4){z, z, z, z});
      }
    }
    SF(22, 4);
    return;
  }
  __builtin_amdgcn_s_setprio(3);
  f32x4 acc[4][4];
#pragma unroll
  for (int i = 0; i < 4; i++)
#pragma unroll
    for (int m = 0; m < 4; m++) { acc[i][m] = (f32x4){0, 0, 0, 0}; asm volatile("" : "+v"(acc[i][m])); }
  u32x4 wf[2];
  {
    LV
    TA_DEF
    unsigned xa[2], fa = lds0 + K_FULL;
#pragma unroll
    for (int b = 0; b < 2; b++) xa[b] = K_X + rot * 8192 + b * 4096 + g4 * 256 + ((c15 ^ (g4 + 4 * b)) * 16);
    u32x4 xf[2][4], wq[2][4];
    lwait_<1>(lds0 + K_FULL, 12u);
    SF(3, 0);
#pragma unroll
    for (int m = 0; m < 4; m++) xf[0][m] = *(const u32x4*)(sm + xa[0] + m * 1024);
#pragma unroll
    for (int i = 0; i < 4; i++) wq[0][i] = trrd2(TA(i), TA(i) + 144);
    unsigned fl;
    asm volatile("ds_read_b32 %0, %1 offset:4" : "=v"(fl) : "v"(fa) : "memory");
    int xg = rot;
#pragma nounroll
    for (int it = 0; it < 3; it++) {
#pragma unroll
      for (int j = 0; j < 6; j++) {
        const int cb = j & 1, nb = cb ^ 1, so = ((j + 1) % 3) * K_SL;
#pragma unroll
        for (int n = 0; n < 16; n++) {
          const int i = n >> 2, m = n & 3;
          acc[i][m] = i == 1 ? mfma16(xf[cb][m], wq[cb][i], acc[i][m]) : mfma16(wq[cb][i], xf[cb][m], acc[i][m]);
          if (n == 0) {
            __builtin_amdgcn_s_waitcnt(0xC07F);
            asm volatile("s_mov_b64 exec, 1\n\tds_inc_u32 %1, %1 offset:%2\n\ts_mov_b64 exec, -1\n\t"
                         "v_cmp_lt_u32 vcc, 3, %0\n\ts_cbranch_vccnz 2f\n"
                         "1:\n\ts_sleep 1\n\tds_read_b32 %0, %1 offset:%3\n\ts_waitcnt lgkmcnt(0)\n\tv_cmp_lt_u32 vcc, 3, %0\n\ts_cbranch_vccz 1b\n2:"
                         : "+v"(fl) : "v"(fa), "n"(128 + 4 * j), "n"(4 * j + 4) : "vcc", "memory");
          }
          if (n == 1) {
            if (j == 2 || j == 5) {
#pragma unroll
              for (int a = 0; a < 2; a++) { ta[a] += j == 2 ? 3 * K_SL : -3 * K_SL; asm volatile("" : "+v"(ta[a])); }
            }
            if (j & 1) {
              const int dx = xg == 8 ? -65536 : 8192;
              xg = xg == 8 ? 0 : xg + 1;
              xa[0] += dx; xa[1] += dx;
              asm volatile("" : "+v"(xa[0]), "+v"(xa[1]));
            }
          }
          if (n >= 2 && n < 6) wq[nb][n - 2] = trrd2(TA(n - 2) + so, TA(n - 2) + so + 144);
          if (n >= 6 && n < 8) {
#pragma unroll
            for (int m2 = 2 * (n - 6); m2 < 2 * (n - 6) + 2; m2++) xf[nb][m2] = *(const u32x4*)(sm + xa[nb] + m2 * 1024);
          }
          if (n == 11) asm volatile("ds_read_b32 %0, %1 offset:%2" : "=v"(fl) : "v"(fa), "n"(4 * j + 8) : "memory");
          if (n == 9 && j == 5) { WF_LD }
          __builtin_amdgcn_sched_barrier(0);
        }
      }
      fa += 24;
      asm volatile("" : "+v"(fa));
      if (it == 0) SF(4, 0);
      if (it == 1) SF(5, 0);
    }
  }
  SF(6, 0);
  {
    LV
    char* sp = sm + K_X + (rot + h >= 9 ? rot + h - 9 : rot + h) * 8192;
    const f32x4 z = {0, 0, 0, 0};
    unsigned xx[4][2];
#pragma unroll
    for (int m = 0; m < 4; m++) {
      xx[m][0] = pk2(acc[3][m][0], acc[3][m][1]); xx[m][1] = pk2(acc[3][m][2], acc[3][m][3]);
      const u32x4 ak = {pk2(acc[0][m][0], acc[0][m][1]), pk2(acc[0][m][2], acc[0][m][3]), xx[m][0], xx[m][1]};
      const f32x4 p0 = mfma16((u32x4){wf[0][0], wf[0][1], g4 == 0 ? wf[0][2] : 0u, g4 == 0 ? wf[0][3] : 0u}, ak, z);
      const f32x4 p1 = mfma16((u32x4){wf[1][0], wf[1][1], g4 == 0 ? wf[1][2] : 0u, g4 == 0 ? wf[1][3] : 0u}, ak, z);
      *(f32x4*)(sp + g4 * 1024 + (m * 16 + c15) * 16) = p0;
      if (g4 == 0) *(f32x4*)(sp + 4096 + (m * 16 + c15) * 16) = p1;
      if (g4 == 1) *(u32x2*)(sp + 6144 + (m * 16 + c15) * 8) = (u32x2){xx[m][0], xx[m][1]};
    }
    asm volatile("" ::: "memory");
    f32x4 pv[5];
#pragma unroll
    for (int i = 0; i < 5; i++) pv[i] = *(const f32x4*)(sp + i * 1024 + l * 16);
    __builtin_amdgcn_sched_barrier(0);
    {
      const __amdgpu_buffer_rsrc_t pb = __builtin_amdgcn_make_buffer_rsrc(act + O_PQ + (size_t)((rt * 8 + g) * 4 + h) * 5120, 0, 5120, 0x00020000);
#pragma unroll
      for (int m = 0; m < 4; m++) {
        const u32x4 aq = {pk2(acc[2][m][0], acc[2][m][1]), pk2(acc[2][m][2], acc[2][m][3]), xx[m][0], xx[m][1]};
        const f32x4 p0 = mfma16((u32x4){wf[0][0], wf[0][1], g4 == 2 ? wf[0][2] : 0u, g4 == 2 ? wf[0][3] : 0u}, aq, z);
        const f32x4 p1 = mfma16((u32x4){wf[1][0], wf[1][1], g4 == 2 ? wf[1][2] : 0u, g4 == 2 ? wf[1][3] : 0u}, aq, z);
        __builtin_amdgcn_raw_buffer_store_b128(__builtin_bit_cast(u32x4, p0), pb, g4 * 1024 + c15 * 16, m * 256, 16);
        __builtin_amdgcn_raw_buffer_store_b128(__builtin_bit_cast(u32x4, p1), pb, g4 * 1024 + c15 * 16 + 4096, m * 256, 16);
      }
    }
    __builtin_amdgcn_sched_barrier(0);
    SF(7, 0);
    {
      float p[18];
#pragma unroll
      for (int f = 0; f < 18; f++) p[f] = pv[f >> 2][f & 3];
      unsigned q[18];
      feat18(p, 1.f, q);
      feat18(p, -1.f, q + 9);
      char* d = sp + l * 96;
#pragma unroll
      for (int i = 0; i < 4; i++) *(u32x4*)(d + i * 16) = (u32x4){q[4 * i], q[4 * i + 1], q[4 * i + 2], q[4 * i + 3]};
      *(u32x2*)(d + 64) = (u32x2){q[16], q[17]};
    }
    asm volatile("" ::: "memory");
    SF(8, 0);
    const unsigned pl = (unsigned)(uintptr_t)(__attribute__((address_space(3))) char*)sp, tk = 4 * g4 + ((l >> 2) & 3);
    const unsigned pa = pl + tk * 96 + (l & 3) * 8, pe = pl + 6144 + tk * 8;
    f32x4 e0[3], e1[3];
#pragma unroll
    for (int ft = 0; ft < 3; ft++) { e0[ft] = z; e1[ft] = z; }
#pragma unroll
    for (int u = 0; u < 2; u++) {
      u32x4 va = {pk2(acc[1][2 * u][0], acc[1][2 * u][1]), pk2(acc[1][2 * u][2], acc[1][2 * u][3]),
                  pk2(acc[1][2 * u + 1][0], acc[1][2 * u + 1][1]), pk2(acc[1][2 * u + 1][2], acc[1][2 * u + 1][3])};
      u32x4 vb = trrd2(pe + u * 256, pe + u * 256 + 128);
#pragma unroll
      for (int i = 0; i < 4; i++) {
        if (c15 < 2 * par) va[i] = 0x3F803F80u;
        if (c15 >= 2 + 2 * par) vb[i] = 0x3F803F80u;
      }
#pragma unroll
      for (int ft = 0; ft < 3; ft++) {
        const unsigned a = pa + ft * 32 + u * 3072;
        const u32x4 ph = trrd2(a, a + 1536);
        e0[ft] = mfma16(va, ph, e0[ft]);
        e1[ft] = mfma16(vb, ph, e1[ft]);
      }
    }
    char* kb = act + O_KVP + (g * 23 * 32 + rt) * 512;
#pragma unroll
    for (int ft = 0; ft < 3; ft++) {
      u32x4 u0 = __builtin_bit_cast(u32x4, e0[ft]), u1 = __builtin_bit_cast(u32x4, e1[ft]);
#pragma unroll
      for (int i = 0; i < 4; i++) { u0[i] = min(u0[i], SENT - 1); u1[i] = min(u1[i], SENT - 1); }
      const int q0 = h * 180 + (ft < 2 ? ft * 64 + l : 128 + g4 * 4 + c15), q1 = h * 180 + 144 + ft * 16 + c15;
      if (ft < 2 || c15 < 4) st16s(kb, (q0 >> 5) * 16384 + (q0 & 31) * 16, u0);
      if (g4 == 0 && (ft < 2 || c15 < 4)) st16s(kb, (q1 >> 5) * 16384 + (q1 & 31) * 16, u1);
    }
    SF(9, 0);
    SF(12, 0);
  }
}

extern "C" __global__ __launch_bounds__(768) void hh_mega_k(const u16* __restrict__ WA, const u16* __restrict__ WO,
                                                            u16* WF, char* __restrict__ act,
                                                            unsigned* __restrict__ flg, const u16* __restrict__ b2,
                                                            u16* __restrict__ out, const u16* __restrict__ x0,
                                                            const u16* __restrict__ w2) {
  __shared__ __attribute__((aligned(16))) char sm[LDS_B];
  const int w_ = __builtin_amdgcn_readfirstlane(threadIdx.x >> 6);
  const int xcd = blockIdx.x & 7, idx = blockIdx.x >> 3;
  const int g_ = (xcd & 1) * 4 + (idx >> 3), rt_ = (xcd >> 1) * 8 + (idx & 7);
#define PV                                                                                           \
  int l, w = w_, g = g_, rt = rt_;                                                                   \
  asm volatile("v_mbcnt_lo_u32_b32 %0, -1, 0\n\tv_mbcnt_hi_u32_b32 %0, -1, %0" : "=v"(l));           \
  __builtin_assume(l >= 0 && l < 64);                                                                \
  asm volatile("" : "+s"(w), "+s"(g), "+s"(rt));                                                     \
  __builtin_assume(w >= 0 && w < 12 && g >= 0 && g < 8 && rt >= 0 && rt < 32);                       \
  const int c15 = l & 15, g4 = l >> 4, tid = w * 64 + l;                                             \
  const unsigned l16 = l * 16;                                                                       \
  (void)c15; (void)g4; (void)tid; (void)l16;
  const unsigned lds0 = (unsigned)(uintptr_t)(__attribute__((address_space(3))) char*)sm;
  int ly = 0;
  STW(0, 0);
  u32x4 wa[27];
#define WA_LD(I, SO) wa[I] = __builtin_amdgcn_raw_buffer_load_b128(war, l16, (SO) + ((I) % 18) * 14336, 0);
#define WA_RANGE(I0, I1, X)                                                                          \
      if (!ly) {                                                                                     \
        const int wlo = w < 4 ? (w & 1) * 9 * 14336 + (w >> 1) * 2048 : (w < 8 ? w + 1 : 4) * 1024;  \
        const int whi = w < 4 ? wlo + 1024 - 9 * 14336 : wlo;                                        \
        _Pragma("unroll") for (int i = I0; i < I1; i++) { WA_LD(i, i < 9 ? wlo : whi) }              \
        VMW((I1) - (I0) + (X));                                                                      \
      } else {                                                                                       \
        _Pragma("unroll") for (int i = I0; i < I1; i++) asm volatile("" : "=v"(wa[i]));              \
        VMW(X);                                                                                      \
      }                                                                                              \
      __builtin_amdgcn_sched_barrier(0);
#define CODE_TOUCH                                                                                   \
      {                                                                                              \
        unsigned t_;                                                                                 \
        asm volatile("s_cmp_lg_u32 %1, %5\n\ts_cbranch_scc1 1f\n\t"                                   \
                     "s_getpc_b64 s[22:23]\n\ts_add_u32 s22, s22, hh_end_k@rel32@lo+4\n\t"            \
                     "s_getpc_b64 s[20:21]\n\ts_add_u32 s20, s20, hh_mega_k@rel32@lo+4\n\ts_addc_u32 s21, s21, hh_mega_k@rel32@hi+12\n\t" \
                     "s_sub_i32 s22, s22, s20\n\ts_sub_i32 s22, s22, 4\n\ts_cmp_lt_i32 s22, 1\n\ts_cbranch_scc1 1f\n\t" \
                     "v_lshlrev_b32 %0, 3, %2\n\tv_add_u32 %0, %3, %0\n\tv_min_i32 %0, s22, %0\n\ts_mov_b32 m0, %4\n\ts_nop 0\n\t" \
                     "global_load_lds_dword %0, s[20:21]\n1:"                                        \
                     : "=&v"(t_) : "s"(w + 16 * ly), "v"(l16), "s"((rt & 7) * 8192), "s"(lds0 + FA + ((g ^ 2) & 6) * 128), "n"(CTW) \
                     : "s20", "s21", "s22", "s23", "scc", "memory");                                 \
      }
#define GATHER(N, SRC, DST) _Pragma("unroll") for (int i = 0; i < N; i++) glds16((SRC) + i * 1024, l16, lds0 + (DST) + i * 1024);
#define KVH 1
#define KV_LD(A_)                                                                                    \
      const __amdgpu_buffer_rsrc_t kr = __builtin_amdgcn_make_buffer_rsrc((A_) + O_KVP + (g * 23 + rt) * 16384, 0, 16384, 0x00020000); \
      const unsigned vo = (l >> 3) * 512 + (8 * w + (l & 7)) * 16;
#define KV_SUM(A_)                                                                                   \
      f32x4 s = (__builtin_bit_cast(f32x4, v[0]) + __builtin_bit_cast(f32x4, v[1])) + (__builtin_bit_cast(f32x4, v[2]) + __builtin_bit_cast(f32x4, v[3])); \
      _Pragma("unroll") for (int e = 0; e < 4; e++) s[e] = xsum(s[e] + dppf<0x128>(s[e]));           \
      _Pragma("unroll") for (int i = 0; i < NC; i += 8)                                               \
        if (l < 8 * NC) st8s((A_) + O_KVS + g * NC * 6144, (i + (l >> 3)) * 6144 + (rt * 32 + 8 * w + (l & 7)) * 8, (u32x2){min(pk2(s[0], s[1]), SENT - 1), min(pk2(s[2], s[3]), SENT - 1)});
#define W2D 0
#define W2F 1
#define W2P 0
#define W2E 0
  u32x4 pq[5];
  u32x2 rw[2][8];
#define PQ_KILL                                                                                      \
      _Pragma("unroll") for (int i = 0; i < 5; i++) asm volatile("" : "=v"(pq[i]));                  \
      if (W2E && W2E < 3) { _Pragma("unroll") for (int i = 0; i < 16; i++) asm volatile("" : "=v"(rw[i >> 3][i & 7])); }
#define W2_LD                                                                                        \
      {                                                                                              \
        const int cq = ((l & 3) << 2) | ((l >> 2) & 3), j = w & 3;                                   \
        _Pragma("unroll") for (int i = 0; i < 2; i++) {                                              \
          const int u = min(rt, 26) * 6 + min(j + 4 * i, 5);                                         \
          const int kb = u / 9, cb = u % 9, th = kb / 6, q = kb % 6;                                 \
          const int t0 = q < 4 ? 3 * (th * 4 + q) : 3 * (th * 4 + (q - 4) * 2) + 2;                  \
          const __amdgpu_buffer_rsrc_t rs = __builtin_amdgcn_make_buffer_rsrc((void*)(w2 + (size_t)(g * 576 + t0 * 16) * D_ + cb * 64), 0, (i && j >= 2) || w < 8 || rt >= 27 ? 0 : 0x7fffffff, 0x00020000); \
          _Pragma("unroll") for (int e = 0; e < 8; e++)                                              \
            rw[i][e] = __builtin_amdgcn_raw_buffer_load_b64(rs, (g4 * 4 * D_ + 4 * cq) * 2 + (e & 3) * 2 * D_, (e >> 2) * (q < 4 ? 32 : 96) * D_, 0); \
        }                                                                                            \
      }
#define W2_ST                                                                                        \
      {                                                                                              \
        const int j = w & 3;                                                                         \
        if (W2F) { _Pragma("unroll") for (int e = 0; e < 8; e++) asm volatile("" : "+v"(rw[1][e])); } \
        __builtin_amdgcn_sched_barrier(0);                                                           \
        _Pragma("unroll") for (int i = 0; i < 2; i++) {                                              \
          if (i && j >= 2) break;                                                                    \
          u32x4 o[4];                                                                                \
          tr4(rw[i], o, l);                                                                          \
          _Pragma("unroll") for (int f = 0; f < 4; f++)                                              \
            st16s((char*)WF + (size_t)g * WF_CH + 19 * 36864 + (rt * 6 + j + 4 * i) * 4096 + f * 1024, l16, o[f]); \
        }                                                                                            \
        STW(9, 8);                                                                                   \
      }
#define W2_PUB { VMW(0); publish(C_WFP, 4, flg + FL_N + g * 32 + rt); }
  {
    PV
    u32x4 v[4];
    const bool red = KVH && w < 4 && rt * 32 + w * 8 < 720;
    if (red) {
      KV_LD(act)
#pragma unroll
      for (int i = 0; i < 4; i++) v[i] = __builtin_amdgcn_raw_buffer_load_b128(kr, vo, i * 4096, 16);
    }
    {
      const __amdgpu_buffer_rsrc_t pr = __builtin_amdgcn_make_buffer_rsrc(act + O_PQ + (size_t)((rt * 8 + g) * 4 + (w & 3)) * 5120, 0, w < 8 ? 0 : 5120, 0x00020000);
#pragma unroll
      for (int i = 0; i < 5; i++) pq[i] = __builtin_amdgcn_raw_buffer_load_b128(pr, l16, i * 1024, 0);
    }
    if (W2E == 1) {
      if (w >= 8) W2_LD
      else { _Pragma("unroll") for (int i = 0; i < 16; i++) asm volatile("" : "=v"(rw[i >> 3][i & 7])); }
    }
    if (w >= 4 && w < 8) {
      const unsigned z = zero();
#pragma unroll
      for (int i = 0; i < 4; i++) *(u32x4*)(sm + (w - 4) * 4096 + i * 1024 + l16) = (u32x4){z, z, z, z};
    }
    bar();
    if (W2E == 2) {
      if (w >= 8) W2_LD
      else { _Pragma("unroll") for (int i = 0; i < 16; i++) asm volatile("" : "=v"(rw[i >> 3][i & 7])); }
    }
    if (red) {
      __builtin_amdgcn_s_setprio(3);
      STW(12, 0);
      KV_SUM(act)
      STW(13, 0);
    }
  }
#pragma nounroll
  for (ly = 0; ly < 2; ly++) {
    char* const A = act + (size_t)ly * LAY_B;
    char* const AR = A;
    unsigned* const fl = flg + (ly * 4 * 32 + rt_) * 256 * FC;
    const unsigned n1 = ly + 1;
    const __amdgpu_buffer_rsrc_t war = __builtin_amdgcn_make_buffer_rsrc((void*)((const char*)WA + (size_t)g_ * 18 * 14336), 0, 18 * 14336, 0x00020000);
    u32x4 bb[2];
#define X_GEMM(N, K0, NR, ...)                                                                       \
      {                                                                                              \
        const char* ap = sm + RA + l16 + m * 2 * SL_B + (K0) * 1024;                                 \
        u32x4 xr[NR];                                                                                \
        _Pragma("unroll") for (int i = 0; i < NR; i++) xr[i] = *(const u32x4*)(ap + i * 1024);       \
        __builtin_amdgcn_sched_barrier(0);                                                           \
        _Pragma("unroll") for (int ks = 0; ks < N; ks++) {                                           \
          const u32x4 xa = xr[ks % NR];                                                              \
          __VA_ARGS__                                                                                \
          if (ks + NR < N) xr[ks % NR] = *(const u32x4*)(ap + (ks + NR) * 1024);                     \
          __builtin_amdgcn_sched_barrier(0);                                                         \
        }                                                                                            \
      }
    if (ly || DRY) {
    PV
    if (!ly) w ^= 4;
    if (w < (ly ? 4 : 1)) {
      if (ly) STW(1, 0);
      if (ly) __builtin_amdgcn_s_setprio(PKPRI);
      unsigned da[2];
#pragma unroll
      for (int t = 0; t < 2; t++) {
        const int vc = (w >> 1) * 32 + t * 16 + c15, hh = (vc * 57) >> 10;
        da[t] = ((w & 1) ? L_PK2 : L_PK + hh * (PK_H - 18 * PK_STR * 4)) + vc * PK_STR * 4 + g4 * 16;
      }
      const unsigned xb = RA + (w & 1) * SL_B + l16, ca = lds0 + L_CNT + 4 * C_X0 + (l & 3) * 4;
      unsigned done = ~15u, cv = 0;
      int m = pick(ca, 2 * ly, done, cv);
      TM(0);
      u32x4 xr[2][9];
#pragma unroll
      for (int i = 0; i < 9; i++) xr[0][i] = *(const u32x4*)(sm + xb + m * 2 * SL_B + i * 1024);
      __builtin_amdgcn_sched_barrier(0);
#pragma unroll
      for (int it = 0; it < 4; it++) {
        f32x4 a0 = {0, 0, 0, 0}, a1 = {0, 0, 0, 0};
        const int mc = m;
        unsigned ap = 0;
#pragma unroll
        for (int ks = 0; ks < 9; ks++) {
          if (ks == 0 && it < 3) asm volatile("ds_read_b32 %0, %1" : "=v"(cv) : "v"(ca) : "memory");
          a0 = mfma16(xr[it & 1][ks], wa[ks], a0);
          a1 = mfma16(xr[it & 1][ks], wa[9 + ks], a1);
          if (ks == PKS && it < 3) {
            asm volatile("s_waitcnt lgkmcnt(0)" : "+v"(cv));
            m = pick(ca, 2 * ly, done, cv);
            if (it == 2) TM(1);
            ap = xb + m * 2 * SL_B;
          }
          if (ks >= PKS && it < 3) {
#pragma unroll
            for (int i = 3 * (ks - PKS); i < 3 * (ks - PKS) + 3 && i < 9; i++) xr[(it + 1) & 1][i] = *(const u32x4*)(sm + ap + i * 1024);
          }
          __builtin_amdgcn_sched_barrier(0);
        }
        *(f32x4*)(sm + da[0] + mc * 64) = a0;
        *(f32x4*)(sm + da[1] + mc * 64) = a1;
        __builtin_amdgcn_sched_barrier(0);
      }
    }
    if (!DRY) { PQ_KILL }
    }
    if (ly) {
    PV
    if (w >= 4 && w < 8) {
      __builtin_amdgcn_s_setprio(VPRI);
      const unsigned da = L_V + (w * 16 + c15 - 56) * VT_STR * 2 + g4 * 16, ca = lds0 + L_CNT + 4 * C_X0 + (l & 3) * 4;
      unsigned done = ~15u;
#pragma nounroll
      for (int it = 0; it < 4; it++) {
        unsigned cv = 0;
        const int m = pick(ca, 2, done, cv);
        f32x4 acc = {0, 0, 0, 0};
        X_GEMM(18, 0, VR, acc = mfma16(xa, wa[ks], acc);)
        *(u32x2*)(sm + da + (m >> 1) * 64 + (m & 1) * 8) = (u32x2){pk2(acc[0], acc[1]), pk2(acc[2], acc[3])};
      }
    }
    PQ_KILL
    }
    if (ly) {
    PV
    if (w >= 8) {
      const int j = w - 8;
      {
        __builtin_amdgcn_s_setprio(3);
        const int m = j;
        f32x4 a8 = {0, 0, 0, 0}, a9 = {0, 0, 0, 0};
        lwait(C_X0 + j, 2);
        X_GEMM(18, 0, XR, if (ks & 1) a9 = mfma16(xa, wa[ks], a9); else a8 = mfma16(xa, wa[ks], a8);)
        a8 += a9;
        if (j == 3) ((unsigned*)(sm + L_V + 72 * VT_STR * 2))[l & 31] = 0x3F803F80u;
        if (c15 < 8) *(f32x4*)(sm + L_PK + 3 * PK_H + ((10 + c15) * PK_STR + j * 16 + 4 * g4) * 4) = a8;
        else *(u32x2*)(sm + L_V + (c15 - 8) * VT_STR * 2 + (j >> 1) * 64 + g4 * 16 + (j & 1) * 8) = (u32x2){pk2(a8[0], a8[1]), pk2(a8[2], a8[3])};
        lsig(C_K1);
        STW(9, 8);
        __builtin_amdgcn_s_setprio(0);
        __builtin_amdgcn_sched_barrier(0);
#pragma unroll
        for (int i = 0; i < 27; i++) { WA_LD(i, i < 18 ? (w + 1) * 1024 : 13312) }
        __builtin_amdgcn_sched_barrier(0);
        STW(0, 8);
      }
      if (QDL == 1) lwait(C_K1, 8 + DRY);
      if (QDL == 2) lwait(C_SM, 4);
      if (QDL >= 3) lwait(C_S2, 4 + DRY);
#pragma nounroll
      for (int m = 0; m < 4; m++) {
        f32x4 acc = {0, 0, 0, 0};
        lwait(C_X0 + m, 2);
        X_GEMM(18, 0, XR, acc = mfma16(xa, wa[ks], acc);)
        *(f32x4*)(sm + L_PQ + ((j * 16 + c15) * PK_STR + m * 16 + 4 * g4) * 4) = acc;
      }
      {
        const int m = j;
        f32x4 ae = {0, 0, 0, 0}, ao = {0, 0, 0, 0};
        X_GEMM(18, 0, XR, if (ks & 1) ao = mfma16(xa, wa[18 + ks / 2], ao); else ae = mfma16(xa, wa[18 + ks / 2], ae);)
#pragma unroll
        for (int e = 0; e < 4; e++) ae[e] += dppf<0x128>(ao[e]);
        if (c15 < 8) *(f32x4*)(sm + L_PQ + ((64 + c15) * PK_STR + m * 16 + 4 * g4) * 4) = ae;
      }
      lsig(C_PQ);
      STW(7, 8);
    }
    PQ_KILL
    }
    {
    PV
    if (w < 8) {
      if (ly && w >= 4) lsig(C_SM);
      if (ly) STW(30, 4);
      if (ly ? w < 4 : DRY && w == 4) {
        const int w = w_ & 3;
        lsig(C_K1);
        if (PKPRI != 3) __builtin_amdgcn_s_setprio(3);
        TM(2);
        char* sp = sm + L_PK + w * PK_H;
        lwait(C_K1, (8 + DRY) * ly);
        TM(3);
        {
          float p[18];
#pragma unroll
          for (int f = 0; f < 18; f++) p[f] = ((const float*)sp)[f * PK_STR + l];
          const float* s2 = (const float*)(sm + L_PK2) + 18 * w * PK_STR + l;
          unsigned q[18];
          if (FV) {
            const float c = 1.44269504088896f;
#pragma unroll
            for (int f = 0; f < 18; f++) p[f] = smul(p[f], c);
#pragma unroll
            for (int f = 0; f < 10; f++) p[f] = sfma(s2[f * PK_STR], c, p[f]);
            if (w < 3) {
#pragma unroll
              for (int f = 10; f < 18; f++) p[f] = sfma(s2[f * PK_STR], c, p[f]);
            }
            feat36(p, q);
          } else {
#pragma unroll
          for (int f = 0; f < 10; f++) p[f] += s2[f * PK_STR];
          if (w < 3) {
#pragma unroll
            for (int f = 10; f < 18; f++) p[f] += s2[f * PK_STR];
          }
          feat18(p, 1.f, q);
          feat18(p, -1.f, q + 9);
          }
          char* d = sp + l * 96;
#pragma unroll
          for (int i = 0; i < 4; i++) *(u32x4*)(d + i * 16) = (u32x4){q[4 * i], q[4 * i + 1], q[4 * i + 2], q[4 * i + 3]};
          *(u32x2*)(d + 64) = (u32x2){q[16], q[17]};
        }
        TM(4);
        if (QDL == 3) lsig(C_S2);
        lwait(C_SM, 4 * ly);
        TM(5);
        const unsigned pl = lds0 + L_PK + w * PK_H + (4 * g4 + ((l >> 2) & 3)) * 96 + (l & 3) * 8;
        const char* v0 = sm + L_V + (18 * w + c15) * VT_STR * 2 + g4 * 16;
        const char* v1 = sm + L_V + (c15 < 2 ? 18 * w + 16 + c15 : 72) * VT_STR * 2 + g4 * 16;
        f32x4 e0[3], e1[3];
#pragma unroll
        for (int ft = 0; ft < 3; ft++) { e0[ft] = (f32x4){0, 0, 0, 0}; e1[ft] = (f32x4){0, 0, 0, 0}; }
#pragma unroll
        for (int u = 0; u < 2; u++) {
          const u32x4 va = *(const u32x4*)(v0 + u * 64), vb = *(const u32x4*)(v1 + u * 64);
#pragma unroll
          for (int ft = 0; ft < 3; ft++) {
            const unsigned a = pl + ft * 32 + u * 3072;
            const u32x4 ph = trrd2(a, a + 1536);
            e0[ft] = mfma16(va, ph, e0[ft]);
            e1[ft] = mfma16(vb, ph, e1[ft]);
          }
        }
        if (QDL == 4) lsig(C_S2);
        const __amdgpu_buffer_rsrc_t kb = __builtin_amdgcn_make_buffer_rsrc(A + O_KVP + (g * 23 * 32 + rt) * 512, 0, ly * 23 * 16384, 0x00020000);
#pragma unroll
        for (int ft = 0; ft < 3; ft++) {
          u32x4 u0 = __builtin_bit_cast(u32x4, e0[ft]), u1 = __builtin_bit_cast(u32x4, e1[ft]);
#pragma unroll
          for (int i = 0; i < 4; i++) { u0[i] = min(u0[i], SENT - 1); u1[i] = min(u1[i], SENT - 1); }
          const int q0 = w * 180 + (ft < 2 ? ft * 64 + l : 128 + g4 * 4 + c15), q1 = w * 180 + 144 + ft * 16 + c15;
          __builtin_amdgcn_raw_buffer_store_b128(u0, kb, ft < 2 || c15 < 4 ? (q0 >> 5) * 16384 + (q0 & 31) * 16 : 0x40000000, 0, 16);
          __builtin_amdgcn_raw_buffer_store_b128(u1, kb, g4 == 0 && (ft < 2 || c15 < 4) ? (q1 >> 5) * 16384 + (q1 & 31) * 16 : 0x40000000, 0, 16);
        }
      }
      STW(6, 0);
      if (w < 4) {
        __builtin_amdgcn_s_setprio(3);
        unsigned la[2];
#pragma unroll
        for (int i = 0; i < 2; i++) {
          const int q = 2 * min(tid + i * 256, 359), h = q / 180, r = q % 180;
          const int f = r < 128 ? (r >> 6) * 16 + (r & 15) : r < 144 ? 32 + (r & 3) : r - 144;
          const int dq = r < 128 ? (r >> 4) & 3 : r < 144 ? (r - 128) >> 2 : 4;
          la[i] = L_KV + ((h * 20 + dq * 4) * VT_STR + (f < 18 ? f : f + 14)) * 2;
        }
        if ((ly || !KVH) && rt * 32 + w * 8 < 720) {
          KV_LD(A)
          u32x4 v[4];
#pragma nounroll
          for (unsigned sp = 0; sp < SPIN_CAP; sp++) {
#pragma unroll
            for (int i = 0; i < 4; i++) v[i] = __builtin_amdgcn_raw_buffer_load_b128(kr, vo, i * 4096, 16);
            if (__all(max(max(umax4(v[0]), umax4(v[1])), max(umax4(v[2]), umax4(v[3]))) != SENT)) break;
            asm volatile("" ::: "memory");
          }
          STW(12, 0);
          KV_SUM(A)
          STW(13, 0);
        }
        {
          const __amdgpu_buffer_rsrc_t kr = __builtin_amdgcn_make_buffer_rsrc(A + O_KVS + (g * NC + rt * NC / 32) * 6144, 0, 5760, 0x00020000);
          u32x4 v[2];
#pragma nounroll
          for (unsigned sp = 0; sp < SPIN_CAP; sp++) {
#pragma unroll
            for (int i = 0; i < 2; i++) v[i] = __builtin_amdgcn_raw_buffer_load_b128(kr, min(tid + i * 256, 359) * 16, 0, 16);
            if (__all(max(umax4(v[0]), umax4(v[1])) != SENT)) break;
            asm volatile("" ::: "memory");
          }
#pragma unroll
          for (int i = 0; i < 2; i++) {
            unsigned* d = (unsigned*)(sm + la[i]);
            d[0] = __builtin_amdgcn_perm(v[i][2], v[i][0], 0x05040100u);
            d[VT_STR / 2] = __builtin_amdgcn_perm(v[i][2], v[i][0], 0x07060302u);
            d[VT_STR] = __builtin_amdgcn_perm(v[i][3], v[i][1], 0x05040100u);
            d[3 * VT_STR / 2] = __builtin_amdgcn_perm(v[i][3], v[i][1], 0x07060302u);
          }
          lsig(C_KV);
          STW(14, 0);
        }
      }
      u32x4 wx[9];
      u32x2 rex;
      {
        const __amdgpu_buffer_rsrc_t wr = __builtin_amdgcn_make_buffer_rsrc((void*)WO, 0, w < 4 ? 0 : (WO_X + 96) * 1024, 0x00020000);
#pragma unroll
        for (int i = 0; i < 9; i++) wx[i] = __builtin_amdgcn_raw_buffer_load_b128(wr, l16, (WO_X + g * 12 + i) * 1024, 0);
        const __amdgpu_buffer_rsrc_t xr = __builtin_amdgcn_make_buffer_rsrc(ly ? (void*)(A + O_XF) : (void*)x0, 0, RXZ && w < 4 ? 0 : ACT_B, 0x00020000);
        rex = __builtin_amdgcn_raw_buffer_load_b64(xr, (c15 * D_ + (g4 & 1) * 4) * 2, ((rt * 64 + (w & 3) * 16) * D_ + g * 72 + 64) * 2, 16);
      }
      {
        const int m = w & 3, hp = w >> 2;
        lwait(C_PHQ, 4 * n1);
        u32x4 ph[2][2];
#pragma unroll
        for (int hh = 0; hh < 2; hh++)
#pragma unroll
          for (int ks = 0; ks < 2; ks++)
            ph[hh][ks] = *(const u32x4*)(sm + L_PHQ + (((2 * hp + hh) * 64 + m * 16 + c15) * VT_STR + ks * 32 + g4 * 8) * 2);
        lwait(C_KV, 4 * n1);
        STW(10, 4);
        unsigned ov[2][2], ox[2];
        u32x4 ka[2][2][2];
#pragma unroll
        for (int hh = 0; hh < 2; hh++)
#pragma unroll
          for (int dt = 0; dt < 2; dt++)
#pragma unroll
            for (int ks = 0; ks < 2; ks++)
              ka[hh][dt][ks] = *(const u32x4*)(sm + L_KV + (((2 * hp + hh) * 20 + (hh && !ly ? (dt == 0 ? c15 + 2 : (c15 & 2) ? 0 : 18 + (c15 & 1)) : dt == 0 ? c15 : 16 + (c15 & 3))) * VT_STR + ks * 32 + g4 * 8) * 2);
#pragma unroll
        for (int hh = 0; hh < 2; hh++) {
          f32x4 e0 = {0, 0, 0, 0}, e1 = {0, 0, 0, 0};
#pragma unroll
          for (int ks = 0; ks < 2; ks++) { e1 = mfma16(ka[hh][1][ks], ph[hh][ks], e1); e0 = mfma16(ka[hh][0][ks], ph[hh][ks], e0); }
          const float rden = 1.0f / (e1[2] + 1e-6f);
          ov[hh][0] = pk2(e0[0] * rden, e0[1] * rden); ov[hh][1] = pk2(e0[2] * rden, e0[3] * rden);
          ox[hh] = pk2(e1[0] * rden, e1[1] * rden);
        }
        char* ob = AR + O_OF + (size_t)rt * TILE_B + g * SL_B;
        st16s(ob, (4 * hp + 2 * (g4 & 1) + (g4 >> 1)) * 1024 + (m * 16 + c15) * 16, pair16(ov[0][0], ov[0][1], ov[1][0], ov[1][1]));
        if (g4 == 0) st8s(ob, 8192 + (m * 16 + c15) * 16 + hp * 8, (u32x2){ox[0], ox[1]});
        VMW(0);
        publish8(C_OPUB, 8 * n1, fl + FH_ + g * 32);
        STW(11, 0);
        lwait(C_OPUB, 8 * n1);
        unsigned t0 = 0, t1 = 0;
        poll2(fl + FH_ + (PRV ? g * 256 : 0) + w * 32, 0, t0, t1);
        STW(15, 0);
        GATHER(9, AR + O_OF + (size_t)rt * TILE_B + w * SL_B, RA + w * SL_B)
        VMW(0);
        KEEP
        lsig(C_O);
        STW(16, 0);
      }
      if (w >= 4) {
        const int m = w - 4;
        f32x4 ae = {blo(rex[0]), bhi(rex[0]), blo(rex[1]), bhi(rex[1])}, ao = {0, 0, 0, 0};
        unsigned ab = RA + g4 * 1024 + c15 * 16 + m * 256, ab2 = ab + 9 * 4096;
        asm volatile("" : "+v"(ab), "+v"(ab2));
        lwait(C_O, 8 * n1);
        u32x4 a[18];
#pragma unroll
        for (int ks = 0; ks < 18; ks++) a[ks] = *(const u32x4*)(sm + (ks < 9 ? ab : ab2) + (ks % 9) * 4096);
        __builtin_amdgcn_sched_barrier(0);
#pragma unroll
        for (int ks = 0; ks < 18; ks += 2) { ae = mfma16(wx[ks / 2], a[ks], ae); ao = mfma16(wx[ks / 2], a[ks + 1], ao); }
#pragma unroll
        for (int e = 0; e < 4; e++) {
          u32x2 o = __builtin_amdgcn_permlane32_swap(__float_as_uint(ao[e]), __float_as_uint(ao[e]), false, false);
          ae[e] += __uint_as_float(o[1]);
        }
        char* xb = AR + O_X1 + (size_t)(rt * 8 + g) * X1S_B;
        const unsigned x0_ = pk2(ae[0], ae[1]), x1_ = pk2(ae[2], ae[3]);
        const u32x4 o = pair16(x0_, x1_, x0_, x1_);
        if (g4 == 0) st16s(xb, 8192 + (m * 16 + c15) * 16, o);
        float s = (ae[0] + ae[1]) + (ae[2] + ae[3]);
        u32x2 t = __builtin_amdgcn_permlane16_swap(__float_as_uint(s), __float_as_uint(s), false, false);
        const float mu = (__uint_as_float(t[0]) + __uint_as_float(t[1])) * 0.125f;
        float m2 = 0.f;
#pragma unroll
        for (int e = 0; e < 4; e++) { const float d = ae[e] - mu; m2 += d * d; }
        t = __builtin_amdgcn_permlane16_swap(__float_as_uint(m2), __float_as_uint(m2), false, false);
        m2 = __uint_as_float(t[0]) + __uint_as_float(t[1]);
        if (g4 == 0) st8s(xb, SL_B + 2048 + (m * 16 + c15) * 8, (u32x2){__float_as_uint(mu), __float_as_uint(m2)});
      }
      __builtin_amdgcn_s_setprio(0);
    } else {
      const int j = w - 8;
      float p[18];
#pragma unroll
      for (int f = 0; f < 18; f++) p[f] = __uint_as_float(pq[f >> 2][f & 3]);
      u32x4 wo_[18], res[2];
#define WOL 1
#define WO_LD                                                                                        \
      {                                                                                              \
        const __amdgpu_buffer_rsrc_t wr = __builtin_amdgcn_make_buffer_rsrc((void*)WO, 0, (WO_X + 96) * 1024, 0x00020000); \
        _Pragma("unroll") for (int i = 0; i < 18; i++) wo_[i] = __builtin_amdgcn_raw_buffer_load_b128(wr, l16, ((g * 18 + i) * 4 + j) * 1024, 0); \
        const __amdgpu_buffer_rsrc_t xr = __builtin_amdgcn_make_buffer_rsrc(ly ? (void*)(A + O_XF) : (void*)x0, 0, ACT_B, 0x00020000); \
        _Pragma("unroll") for (int mp = 0; mp < 2; mp++)                                             \
          res[mp] = __builtin_amdgcn_raw_buffer_load_b128(xr, (((g4 & 1) * 16 + c15) * D_ + (g4 >> 1) * 8) * 2, ((rt * 64 + mp * 32) * D_ + g * 72 + j * 16) * 2, 16); \
      }
      if (!WOL) WO_LD
      {
        if (ly) {
          lwait(C_PQ, 4);
          lwait(C_K1, 8 + DRY);
          lwait(C_SM, 4);
          const float* src = (const float*)(sm + L_PQ) + 18 * j * PK_STR + l;
#pragma unroll
          for (int f = 0; f < 18; f++) p[f] = src[f * PK_STR];
        }
#pragma unroll
        for (int sg = 0; sg < 2; sg++) {
          unsigned q[9];
          feat18(p, sg ? -1.f : 1.f, q);
          u32x4* dst = (u32x4*)(sm + L_PHQ + ((j * 64 + l) * VT_STR + sg * 32) * 2);
          dst[0] = (u32x4){q[0], q[1], q[2], q[3]};
          dst[1] = (u32x4){q[4], q[5], q[6], q[7]};
          const unsigned z = zero();
          dst[2] = (u32x4){q[8], z, z, z};
          dst[3] = (u32x4){z, z, z, z};
        }
      }
      lsig(C_PHQ);
      STW(8, 8);
      if (WOL == 2) lwait(C_KV, 4 * n1);
#define W2O 1
      if (W2O) {
        if (!ly && rt < 27) {
          if (W2O == 2) lwait(C_KV, 4);
          W2_LD
        } else {
#pragma unroll
          for (int i = 0; i < 16; i++) asm volatile("" : "=v"(rw[i >> 3][i & 7]));
        }
        __builtin_amdgcn_sched_barrier(0);
      }
      if (WOL == 3 && !ly) lwait(C_OPUB, 8);
      if (WOL == 4 && !ly) lwait(C_OPUB, 1);
      if (WOL) WO_LD
#define W2G 1
      u32x4 ow[2][4];
      if (W2E < 3 && !ly && rt < 27) {
        if (!W2E && !W2O) {
          lwait(C_KV, 4);
          W2_LD
        }
        if (W2G) {
#pragma unroll
          for (int i = 0; i < 2; i++) tr4(rw[i], ow[i], l);
        } else {
          W2_ST
          W2_PUB
        }
      } else {
        if (W2G) {
#pragma unroll
          for (int i = 0; i < 8; i++) asm volatile("" : "=v"(ow[i >> 2][i & 3]));
        }
      }
      __builtin_amdgcn_s_waitcnt(0x0F70);
      __builtin_amdgcn_s_setprio(3);
      f32x4 acc[4];
#pragma unroll
      for (int mp = 0; mp < 2; mp++) {
        u32x2 q0 = __builtin_amdgcn_permlane16_swap(res[mp][0], res[mp][2], false, false);
        u32x2 q1 = __builtin_amdgcn_permlane16_swap(res[mp][1], res[mp][3], false, false);
#pragma unroll
        for (int mm = 0; mm < 2; mm++) acc[2 * mp + mm] = (f32x4){blo(q0[mm]), bhi(q0[mm]), blo(q1[mm]), bhi(q1[mm])};
      }
      {
        unsigned ab = RA + g4 * 1024 + c15 * 16, ab2 = ab + 9 * 4096;
        asm volatile("" : "+v"(ab), "+v"(ab2));
        lwait(W2G == 2 ? C_OPUB : C_O, 8 * n1);
        if (W2G && !ly && rt < 27) {
#pragma unroll
          for (int i = 0; i < 2; i++) {
            if (i && j >= 2) break;
#pragma unroll
            for (int f = 0; f < 4; f++) st16s((char*)WF + (size_t)g * WF_CH + 19 * 36864 + (rt * 6 + j + 4 * i) * 4096 + f * 1024, l16, ow[i][f]);
          }
        }
        if (W2G == 2) lwait(C_O, 8 * n1);
        STW(17, 8);
        u32x4 a[3][4];
#define K2_LD(S) _Pragma("unroll") for (int m = 0; m < 4; m++) a[(S) % 3][m] = *(const u32x4*)(sm + ((S) < 9 ? ab : ab2) + ((S) % 9) * 4096 + m * 256);
        K2_LD(0)
        K2_LD(1)
#pragma unroll
        for (int ks = 0; ks < 18; ks++) {
          if (ks + 2 < 18) { K2_LD(ks + 2) }
          __builtin_amdgcn_sched_barrier(0);
#pragma unroll
          for (int m = 0; m < 4; m++) acc[m] = mfma16(wo_[ks], a[ks % 3][m], acc[m]);
          __builtin_amdgcn_sched_barrier(0);
        }
      }
      STW(18, 8);
      {
        char* xb = AR + O_X1 + (size_t)(rt * 8 + g) * X1S_B;
#pragma unroll
        for (int mp = 0; mp < 2; mp++) {
          const f32x4 v0 = acc[2 * mp], v1 = acc[2 * mp + 1];
          st16s(xb, (2 * j + (g4 >> 1)) * 1024 + ((2 * mp + (g4 & 1)) * 16 + c15) * 16,
                pair16(pk2(v0[0], v0[1]), pk2(v0[2], v0[3]), pk2(v1[0], v1[1]), pk2(v1[2], v1[3])));
        }
        float smu = 0.f, sm2 = 0.f;
#pragma unroll
        for (int m = 0; m < 4; m++) {
          const float mu = xsum((acc[m][0] + acc[m][1]) + (acc[m][2] + acc[m][3])) * (1.f / 16.f);
          float m2 = 0.f;
#pragma unroll
          for (int e = 0; e < 4; e++) { const float d = acc[m][e] - mu; m2 += d * d; }
          m2 = xsum(m2);
          if (g4 == m) { smu = mu; sm2 = m2; }
        }
        st8s(xb, SL_B + j * 512 + l * 8, (u32x2){__float_as_uint(smu), __float_as_uint(sm2)});
      }
    }
    }
    {
      PV
      const int mw = w & 3, th = w >> 2;
      const __amdgpu_buffer_rsrc_t wb = __builtin_amdgcn_make_buffer_rsrc((void*)((const char*)WF + (size_t)g * WF_CH + w * 3072), 0, WF_CH - w * 3072, 0x00020000);
#define F_LDW(S)                                                                                     \
    _Pragma("unroll") for (int i = 0; i < 3; i++)                                                    \
      ring[(S) % RD][i] = __builtin_amdgcn_raw_buffer_load_b128(wb, l16 + i * 1024, fso, WAUX);      \
    asm volatile("s_add_u32 %0, %0, %1" : "+s"(fso) : "n"((S) == 17 ? 0x12000 : 0x9000) : "scc");
#define ST_SLOT(I) (FH + ((I) >> 3) * 24576 + 16384 + ((I) & 7) * 1024)
      int fso = 0;
      u32x4 ring[RD][3];
      __builtin_amdgcn_sched_barrier(0);
#pragma unroll
      for (int s = 0; s < RD; s++) { F_LDW(s) }
      __builtin_amdgcn_sched_barrier(0);
      if (w >= 4) {
        VMW(3 * RD);
        publish8(C_X1PUB, 8 * n1, fl + 2 * FH_ + g * 32);
        if (W2G && !ly && w >= 8 && rt < 27) publish(C_WFP, 4, flg + FL_N + g * 32 + rt);
        STW(19, 8);
      }
      if (W2E >= 3) {
        if (!ly && w >= 8) {
          if (W2D) lwait(C_X1PUB, 8);
          W2_LD
        } else {
#pragma unroll
          for (int i = 0; i < 16; i++) asm volatile("" : "=v"(rw[i >> 3][i & 7]));
        }
        __builtin_amdgcn_sched_barrier(0);
      }
      lwait(C_X1PUB, 8 * n1);
      GATHER(3, (const char*)WF + (size_t)g * WF_CH + 18 * 36864 + w * 3072, FH + (th * 6 + mw) * 4096)
      if (w < 8) {
        unsigned t0 = 0, t1 = 0;
        poll2(fl + 2 * FH_ + (PRV ? g * 256 : 0) + w * 32, 0, t0, t1);
        STW(20, 0);
        const char* sb = AR + O_X1 + (size_t)(rt * 8 + w) * X1S_B;
        GATHER(9, sb, FA + w * SL_B)
#pragma unroll
        for (int i = 0; i < 3; i++) glds16(sb + SL_B + i * 1024, l16, lds0 + ST_SLOT(3 * w + i));
        if (W2E < 3 && w == 3) poll(flg + FL_N + g * 32, min(l, 26));
        VMW(0);
        KEEP
        lsig(C_X1);
      }
      if (W2E == 4 && !ly && w >= 8 && rt < 27) W2_ST
      lwait(C_X1, 8 * n1);
      __builtin_amdgcn_sched_barrier(0);
      STW(21, 0); STC(46, 9);
      if (W2E == 3 && !ly && w >= 8 && rt < 27) { __builtin_amdgcn_s_setprio(3); W2_ST }
      if (w == 11) {
        unsigned sb = l * 8;
        asm volatile("" : "+v"(sb));
        float ma = 0.f, mb = 0.f, M2 = 0.f;
        float t0[40];
#pragma unroll
        for (int i = 0; i < 40; i++) {
          const f32x2 t = *(const f32x2*)(sm + sb + ST_SLOT(3 * (i / 5) + (i % 5) / 2) + (i % 5) % 2 * 512);
          t0[i] = t[0]; M2 += t[1];
          if (i % 5 < 4) ma += t[0]; else mb += t[0];
        }
        asm volatile("" : "+v"(ma)); asm volatile("" : "+v"(mb)); asm volatile("" : "+v"(M2));
        const float mn = ma * (16.f / 576.f) + mb * (8.f / 576.f);
        float ea = 0.f, eb = 0.f;
#pragma unroll
        for (int i = 0; i < 40; i++) { const float e = t0[i] - mn; if (i % 5 < 4) ea += e * e; else eb += e * e; }
        asm volatile("" : "+v"(ea)); asm volatile("" : "+v"(eb));
        const float var = (M2 + 16.f * ea + 8.f * eb) * (1.f / 576.f) + 1e-5f;
        const float sd = __builtin_amdgcn_sqrtf(var);
        unsigned mh, mm, ml, sh, sm_, sl;
        split3(-mn, mh, mm, ml);
        split3(sd, sh, sm_, sl);
        const u32x4 sv = {mh | (mh << 16), mm | (mm << 16), mh | (ml << 16), sh | (sm_ << 16)};
        *(u32x4*)(sm + FE + l16) = sv;
        *(u32x4*)(sm + FE + 1024 + l16) = sv;
        *(u32x4*)(sm + FE + 2048 + l16) = sv;
        ((float*)(sm + L_R2))[l] = 1.0f / var;
        lsig_(lds0 + L_CNT + 4 * C_F0, 4u);
      }
      __builtin_amdgcn_sched_barrier(0);
      if (W2E >= 3 && !ly && w >= 8) {
        if (rt < 27) W2_PUB
        if (w == 8) {
          poll(flg + FL_N + g * 32, min(l, 26));
          lsig(C_WF);
        }
        __builtin_amdgcn_sched_barrier(0);
      }
      f32x4 acc[3][4];
#pragma unroll
      for (int i = 0; i < 3; i++)
#pragma unroll
        for (int m = 0; m < 4; m++) acc[i][m] = (f32x4){0, 0, 0, 0};
#define F_OFF(S) ((S) < 18 ? FA + (S) * 4096 : (S) == 18 ? FE : FH + ((S) - 19) * 4096)
#define F_WIN(S) ((S) < 9 ? FA : (S) < 18 ? FA + 9 * 4096 : (S) == 18 ? FE : (S) < 31 ? 0 : 65536)
#define F_GRP(S) ((S) == 18 ? 0 : (S) == 25 ? 2 : (S) == 31 ? 3 : -1)
#define F_WAIT(C) lwait_<FSLP>(lds0 + L_CNT + 4 * (C_F0 + (C)), 4u * n1);
#define F_LDA(S)                                                                                     \
      if ((S) == 0) { lb = g4 * 1024 + c15 * 16 + FA; asm volatile("" : "+v"(lb)); }                 \
      else if ((S) == 31) { lb = l16 + 65536; asm volatile("" : "+v"(lb)); }                         \
      else if ((S) < 19 && F_WIN(S) != F_WIN((S) - 1)) { lb += F_WIN(S) - F_WIN((S) - 1); asm volatile("" : "+v"(lb)); } \
      _Pragma("unroll") for (int m = 0; m < 4; m++)                                                  \
        a[(S) & 1][m] = *(const u32x4*)(sm + (F_WIN(S) ? lb : l16) + (F_OFF(S) - F_WIN(S)) + m * ((S) < 19 ? 256 : 1024));
#define F_STEP                                                                                       \
    {                                                                                                \
      if (CT && s == CT) { CODE_TOUCH }                                                              \
      if (FP2 && s == FP2) setprio(1);                                                               \
      if (FP2 && s == FP3) setprio(0);                                                               \
      if (s != 18 && s != 36 && F_GRP(s + 1) < 0) { F_LDA(s + 1) }                                   \
      __builtin_amdgcn_sched_barrier(0);                                                             \
      _Pragma("unroll") for (int i = 0; i < 3; i++)                                                  \
        _Pragma("unroll") for (int m = 0; m < 4; m++)                                                \
          acc[i][m] = mfma16(s == 18 ? a[1][i] : ring[(s - (s > 18)) % RD][i], a[s & 1][m], acc[i][m]); \
      if (W2E >= 3 && s + RD == 18 && !ly) lwait(C_WF, 1);                                           \
      if (s == 18) {}                                                                                \
      else if (s - (s > 18) + RD < 36) { F_LDW(s - (s > 18) + RD) }                                  \
      __builtin_amdgcn_sched_barrier(0);                                                             \
      if (s == 17) {                                                                                 \
        _Pragma("unroll") for (int i = 0; i < 3; i++) a[1][i] = *(const u32x4*)(sm + FH + (th * 6 + mw) * 4096 + i * 1024 + l16); \
      }                                                                                              \
      if (s != 18 && s != 36 && F_GRP(s + 1) >= 0) { F_WAIT(F_GRP(s + 1)) F_LDA(s + 1) }             \
    }
      u32x4 a[2][4];
      unsigned lb = 0;
      if (FPRI) setprio(FPRI);
      F_LDA(0)
#pragma unroll
      for (int s = 0; s < 19; s++) F_STEP
      {
        STW(22, 0); STW(43, 4); STW(44, 8); STW(45, 11);
#pragma unroll
        for (int i = 0; i < 3; i++)
#pragma unroll
          for (int m = 0; m < 4; m++)
#pragma unroll
            for (int j = 0; j < 4; j++) acc[i][m][j] = __int_as_float(max(__float_as_int(acc[i][m][j]), 0));
        __builtin_amdgcn_sched_barrier(0);
#pragma unroll
        for (int i = 0; i < 3; i++)
#pragma unroll
          for (int m = 0; m < 4; m++) acc[i][m] = acc[i][m] * acc[i][m];
        __builtin_amdgcn_sched_barrier(0);
        char* d0 = sm + FH + (th * 6 + mw) * 4096 + l16;
        char* d1 = sm + FH + (th * 6 + 4 + (mw >> 1)) * 4096 + (mw & 1) * 8 + l16;
#pragma unroll
        for (int m = 0; m < 4; m++) {
          *(u32x4*)(d0 + m * 1024) = (u32x4){pk2(acc[0][m][0], acc[0][m][1]), pk2(acc[0][m][2], acc[0][m][3]),
                                             pk2(acc[1][m][0], acc[1][m][1]), pk2(acc[1][m][2], acc[1][m][3])};
          *(u32x2*)(d1 + m * 1024) = (u32x2){pk2(acc[2][m][0], acc[2][m][1]), pk2(acc[2][m][2], acc[2][m][3])};
        }
#pragma unroll
        for (int i = 0; i < 3; i++)
#pragma unroll
          for (int m = 0; m < 4; m++) acc[i][m] = (f32x4){0, 0, 0, 0};
        lsig(C_F1 + th);
        if (FP2) setprio(2); else if (FPRI) setprio(0);
        F_WAIT(1)
        STW(23, 0);
      }
      F_LDA(19)
#pragma unroll
      for (int s = 19; s < 37; s++) F_STEP
      STW(24, 0); STW(33, 3); STW(34, 4); STW(35, 7); STW(36, 8); STW(37, 11); STC(47, 9);
      char* pb = AR + O_PP + (size_t)rt * 8 * TILE_B + g * SL_B;
      {
        PV
        const int sl = 2 * (g4 & 1) + (c15 >> 3);
#pragma unroll
        for (int i = 0; i < 3; i++) {
          const unsigned dst = sl * TILE_B + (2 * (3 * w + i) + (g4 >> 1)) * 128 + (c15 & 7) * 16;
#pragma unroll
          for (int m = 0; m < 4; m += 2) {
            u32x4 o = pair16(pk2(acc[i][m][0], acc[i][m][1]), pk2(acc[i][m][2], acc[i][m][3]),
                             pk2(acc[i][m + 1][0], acc[i][m + 1][1]), pk2(acc[i][m + 1][2], acc[i][m + 1][3]));
            if (sl + 2 * m != g) st16s(pb + m * 2 * TILE_B, dst, o);
            else *(u32x4*)(sm + FA + (2 * (3 * w + i) + (g4 >> 1)) * 1024 + ((g ^ 1) * 8 + (c15 & 7)) * 16) = o;
          }
        }
#pragma unroll
        for (int i = 0; i < 2; i++) bb[i] = *(const u32x4*)(b2 + min(8 * w + 4 * (g4 >> 1) + 2 * i + (c15 >> 3), 71) * 8);
        __builtin_amdgcn_sched_barrier(0);
        STW(25, 0);
        WA_RANGE(0, WNA, 2)
        publish8(C_PPUB, 12 * n1, fl + 3 * FH_ + g * 32);
        STW(26, 0); STW(38, 3); STW(39, 4); STW(40, 7); STW(41, 8); STW(42, 11);
      }
    }
    {
      PV
      const int r = l & 7;
      unsigned pa[4], xo;
      u32x4 sel;
      {
        const unsigned it = (min(w, 8) * 64 + (g4 >> 1) * 32 + c15) * 16;
#pragma unroll
        for (int q = 0; q < 4; q++) pa[q] = RA + ((g + 1 + 2 * q + (g4 & 1)) & 7) * SL_B + it;
        xo = FA + (it >> 7) * 1024 + (g * 8 + r) * 16;
        if (g4 & 1) pa[3] = xo ^ 128;
#pragma unroll
        for (int q = 0; q < 4; q++) sel[q] = (c15 >> 3) == (g4 >> 1) && (r >> 1) == q ? ((r & 1) ? 0x3F800000u : 0x3F80u) : 0u;
        asm volatile("" : "+v"(pa[0]), "+v"(pa[1]), "+v"(pa[2]), "+v"(pa[3]), "+v"(xo), "+v"(sel));
      }
      const float rs2 = ((const float*)(sm + L_R2))[g * 8 + r];
      if (!(g4 & 1)) { bb[0] = *(const u32x4*)(sm + xo); bb[1] = *(const u32x4*)(sm + xo + 2048); }
      unsigned p31 = pa[3] + ((g4 & 1) ? 2048 : 256);
      f32x4 t[2];
#pragma unroll
      for (int i = 0; i < 2; i++) t[i] = mfma16(sel, bb[i], (f32x4){0, 0, 0, 0});
      asm volatile("" : "+v"(p31), "+v"(t[0]), "+v"(t[1]));
      if (w < 8) lwait(C_PPUB, 12 * n1);
      WA_RANGE(WNA, WNA + WNC, 0)
      unsigned t0 = 0, t1 = 0;
      if (w < 8 && w != g) {
        poll2(fl + 3 * FH_ + (PRV ? g * 256 : 0) + w * 32, 0, t0, t1);
        STW(27, 0);
        GATHER(9, AR + O_PP + (size_t)(rt * 8 + g) * TILE_B + w * SL_B, RA + w * SL_B)
      }
      WA_RANGE(WNA + WNC, WNA + WNC + WNB, 0)
      KEEP
      if (w < 8) lsig(C_P);
      if (w < 9) {
        lwait(C_P, 8 * n1);
        STW(28, 0);
        u32x4 p[2][4];
#pragma unroll
        for (int q = 0; q < 4; q++)
#pragma unroll
          for (int i = 0; i < 2; i++) p[i][q] = *(const u32x4*)(sm + (q == 3 && i ? p31 : pa[q] + i * 256));
        f32x4 d[2] = {{0, 0, 0, 0}, {0, 0, 0, 0}};
#pragma unroll
        for (int q = 0; q < 4; q++)
#pragma unroll
          for (int i = 0; i < 2; i++) d[i] = mfma16(sel, p[i][q], d[i]);
#pragma unroll
        for (int i = 0; i < 2; i++) d[i] = d[i] * rs2 + t[i];
        const u32x4 a = pair16(pk2(d[0][0], d[0][1]), pk2(d[0][2], d[0][3]), pk2(d[1][0], d[1][1]), pk2(d[1][2], d[1][3]));
        f32x2 v[4];
#pragma unroll
        for (int i = 0; i < 4; i++) v[i] = (f32x2){blo(a[i]), bhi(a[i])};
        st16s(ly ? (char*)out : A + LAY_B + O_XF, ((rt * 64 + g * 8 + r) * D_ + (tid >> 3) * 8) * 2, a);
        if (ly == 0) {
          ln_part(v, (f32x2*)(sm + R0), w, l);
          lsig(C_LN);
          lwait(C_LN, 9);
          st16s(A + LAY_B + O_XN + (size_t)(rt * 8 + (g & 6)) * SL_B + (g & 1) * 128, (tid >> 3) * 256 + r * 16, ln_fin(v, (const f32x2*)(sm + R0), r));
          STW(29, 0);
        }
      }
      if (ly == 1) break;
      __builtin_amdgcn_sched_barrier(0);
      WA_RANGE(WNA + WNC + WNB, 18, 0)
#pragma unroll
      for (int i = 18; i < 27; i++) asm volatile("" : "=v"(wa[i]));
      if (w < 9) {
        publish8(C_XNPUB, 9, fl + g * 32);
        STW(30, 0);
      }
      if (w < 8) {
        lwait(C_XNPUB, 9);
        poll2(fl + (PRV ? g * 256 : 0) + (w & 6) * 32, (l & 1) * 32, t0, t1);
        STW(31, 0);
        GATHER(9, A + LAY_B + O_XN + (size_t)rt * TILE_B + w * SL_B, RA + w * SL_B)
        VMW(0);
        KEEP
        lsig(C_X0 + (w >> 1));
      }
      PQ_KILL
    }
  }
}
extern "C" __global__ void hh_end_k() {}

extern "C" void kernel_launch(void* const* d_in, const int* in_sizes, int n_in, void* d_out, int out_size,
                              void* d_ws, size_t ws_size, hipStream_t stream) {
  const u16* x0 = (const u16*)d_in[0];
  const u16* wqkv = (const u16*)d_in[1];
  const u16* wphi = (const u16*)d_in[2];
  const u16* wo = (const u16*)d_in[3];
  const u16* w1 = (const u16*)d_in[5];
  const u16* b1 = (const u16*)d_in[6];
  const u16* w2 = (const u16*)d_in[7];
  const u16* b2 = (const u16*)d_in[8];
  char* ws = (char*)d_ws;
  size_t off = 0;
  auto alloc = [&](size_t b) { void* p = ws + off; off += (b + 4095) & ~(size_t)4095; return p; };
  char* WA = (char*)alloc((size_t)8 * 18 * 14 * 1024);
  char* WO = (char*)alloc((size_t)(WO_X + 96) * 1024);
  char* WF = (char*)alloc((size_t)8 * WF_CH);
  unsigned* flg = (unsigned*)alloc((size_t)FL_N * 4 + 1024);
  char* act = (char*)alloc(O_PQ + (size_t)1024 * 5120);
  hh_first_k<<<256, 768, 0, stream>>>(x0, wqkv, wphi, act, WA, wo, w1, w2, WO, WF, b1, flg);
  hh_mega_k<<<256, 768, 0, stream>>>((const u16*)WA, (const u16*)WO, (u16*)WF, act, flg, b2, (u16*)d_out, x0, w2);
}
```
